```python
import torch

def _softmax(x, axis=-1):
    return torch.softmax(x, dim=axis)

def _gelu(x):
    return torch.nn.functional.gelu(x, approximate='tanh')

def _silu(x):
    return torch.nn.functional.silu(x)

def _sigmoid(x):
    return torch.sigmoid(x)

def _layernorm(x, axis=-1, eps=1e-05):
    m = x.mean(dim=axis, keepdim=True)
    v = x.var(dim=axis, keepdim=True, unbiased=False)
    return (x - m) / torch.sqrt(v + eps)

def _scatter_add(tgt, idx, src, axis=0):
    return tgt.clone().index_add_(axis, idx, src)

def _segment_sum(src, seg, n):
    out = torch.zeros((n,) + src.shape[1:], dtype=src.dtype)
    return out.index_add_(0, seg, src)

def _pad(x, pw):
    flat = tuple((v for pair in reversed(pw) for v in pair))
    return torch.nn.functional.pad(x, flat)
kernel_name = 'sequential_preLN__memory_aug_recurrent_mem_tr__mixer_s_mlp__prompt'

def setup_inputs():

    _w = lambda a: (lambda t: t.to(torch.bfloat16) if t.is_floating_point() else t)(torch.as_tensor(a))

    def _A_setup():
        B, S, Dm = (1, 2048, 512)
        H, Dh = (32, 128)
        M, seg = (8, 32)
        seg = min(seg, S)
        S = S // seg * seg
        return {'x': _w((lambda *s: torch.randn(*s, dtype=torch.bfloat16) * 0.05)(B, S, Dm)), 'mem0': _w((lambda *s: torch.randn(*s, dtype=torch.bfloat16) * 0.2)(B, M, Dm)), 'Wq': _w((lambda *s: torch.randn(*s, dtype=torch.bfloat16) * 1.6)(Dm, H, Dh) * Dm ** (-0.5)), 'Wk': _w((lambda *s: torch.randn(*s, dtype=torch.bfloat16) * 1.6)(Dm, H, Dh) * Dm ** (-0.5)), 'Wv': _w((lambda *s: torch.randn(*s, dtype=torch.bfloat16) * 0.2)(Dm, H, Dh) * Dm ** (-0.5)), 'Wo': _w((lambda *s: torch.randn(*s, dtype=torch.bfloat16) * 0.2)(H, Dh, Dm) * (H * Dh) ** (-0.5)), 'seg_len': seg}
    _A_in = _A_setup()

    def _B_setup():
        B, S, D, F = (1, 2048, 512, 1792)
        return {'x': _w((lambda *s: torch.randn(*s, dtype=torch.bfloat16) * 0.05)(B, S, D)), 'w1': _w((lambda *s: torch.randn(*s, dtype=torch.bfloat16) * 0.8)(D, D) * D ** (-0.5)), 'w2': _w((lambda *s: torch.randn(*s, dtype=torch.bfloat16) * 0.8)(D, D) * D ** (-0.5)), 'w3': _w((lambda *s: torch.randn(*s, dtype=torch.bfloat16) * 0.8)(D, F) * D ** (-0.5)), 'w4': _w((lambda *s: torch.randn(*s, dtype=torch.bfloat16) * 0.8)(F, D) * F ** (-0.5))}
    _B_in = _B_setup()
    return {'A_x': _A_in['x'], 'A_mem0': _A_in['mem0'], 'A_Wq': _A_in['Wq'], 'A_Wk': _A_in['Wk'], 'A_Wv': _A_in['Wv'], 'A_Wo': _A_in['Wo'], 'A_seg_len': _A_in['seg_len'], 'B_x': _B_in['x'], 'B_w1': _B_in['w1'], 'B_w2': _B_in['w2'], 'B_w3': _B_in['w3'], 'B_w4': _B_in['w4']}

def reference(A_x, A_mem0, A_Wq, A_Wk, A_Wv, A_Wo, A_seg_len, B_x, B_w1, B_w2, B_w3, B_w4):

    def _A_ref(x, mem0, Wq, Wk, Wv, Wo, seg_len):
        B, S, Dm = x.shape
        H, Dh = (Wq.shape[1], Wq.shape[2])
        M = mem0.shape[1]
        scale = Dh ** (-0.5)
        n_seg = S // seg_len
        L = M + seg_len
        idx = torch.arange(L)
        mask = (idx[:, None] < M) | (idx[:, None] >= idx[None, :])
        mem = mem0
        outs = []
        for s in range(n_seg):
            xs = x[:, s * seg_len:(s + 1) * seg_len, :].clone()
            h = torch.concatenate([mem, xs], axis=1)
            q = torch.einsum('btd,dhe->bhte', h, Wq)
            k = torch.einsum('btd,dhe->bhte', h, Wk)
            v = torch.einsum('btd,dhe->bhte', h, Wv)
            scores = torch.einsum('bhqe,bhke->bhqk', q, k) * scale
            scores = torch.where(mask[None, None], scores, -1000000000.0)
            p = _softmax(scores, axis=-1)
            o = torch.einsum('bhqk,bhke->bhqe', p, v)
            o = torch.einsum('bhte,hed->btd', o, Wo)
            mem = o[:, :M, :].clone()
            outs.append(o[:, M:, :])
        return torch.concatenate(outs, axis=1)

    def _B_ref(x, w1, w2, w3, w4):
        B, S, D = x.shape
        h = _gelu(_layernorm(x, axis=-1) @ w1)
        c = D // 4
        g0 = _pad(h[:, :, 0 * c:1 * c], ((0, 0), (1, 0), (0, 0)))[:, :S, :]
        g1 = _pad(h[:, :, 1 * c:2 * c], ((0, 0), (0, 1), (0, 0)))[:, 1:, :]
        g2 = _pad(h[:, :, 2 * c:3 * c], ((0, 0), (2, 0), (0, 0)))[:, :S, :]
        g3 = _pad(h[:, :, 3 * c:], ((0, 0), (0, 2), (0, 0)))[:, 2:, :]
        hs = torch.concatenate([g0, g1, g2, g3], axis=-1) @ w2
        x1 = x + hs
        h2 = _gelu(_layernorm(x1, axis=-1) @ w3) @ w4
        return x1 + h2
    x = A_x
    h = x + _A_ref(_layernorm(x, axis=-1), A_mem0, A_Wq, A_Wk, A_Wv, A_Wo, A_seg_len)
    h2 = h + _B_ref(_layernorm(h, axis=-1), B_w1, B_w2, B_w3, B_w4)
    return h2
```

```cpp
#include <hip/hip_runtime.h>
#include <hip/hip_bf16.h>
#include <stdint.h>

typedef unsigned short u16;
__device__ __forceinline__ float bf2f(u16 v){ return __uint_as_float(((uint32_t)v)<<16); }
__device__ __forceinline__ u16 f2bf(float f){ uint32_t u=__float_as_uint(f); u += 0x7FFF + ((u>>16)&1); return (u16)(u>>16); }
__device__ __forceinline__ float rbf(float f){ return bf2f(f2bf(f)); }
__device__ __forceinline__ float gelu_t(float x){ float u=0.7978845608028654f*(x+0.044715f*x*x*x); return 0.5f*x*(1.f+tanhf(u)); }

__global__ void n_ln(const u16* in, u16* out, int rows){
  int row = blockIdx.x*4 + threadIdx.x/64; int l = threadIdx.x%64;
  if(row>=rows) return;
  float v[8]; float s=0;
  for(int i=0;i<8;i++){ v[i]=bf2f(in[row*512+l*8+i]); s+=v[i]; }
  for(int o=32;o;o>>=1) s+=__shfl_xor(s,o);
  float m=s/512.f; float q=0;
  for(int i=0;i<8;i++){ float d=v[i]-m; q+=d*d; }
  for(int o=32;o;o>>=1) q+=__shfl_xor(q,o);
  float r=rsqrtf(q/512.f+1e-5f);
  for(int i=0;i<8;i++) out[row*512+l*8+i]=f2bf((v[i]-m)*r);
}
__global__ __launch_bounds__(256) void n_gemm(const u16* A, int lda, const u16* B, int ldb, u16* C, int ldc, int M, int N, int K, int mode, const u16* add, int ldadd, int shift){
  __shared__ float As[32][65]; __shared__ float Bs[32][65];
  int tx=threadIdx.x%16, ty=threadIdx.x/16;
  int m0=blockIdx.y*64, n0=blockIdx.x*64;
  float acc[4][4]={};
  for(int k0=0;k0<K;k0+=32){
    for(int i=threadIdx.x;i<64*32;i+=256){
      int r=i/32,c=i%32; int gm=m0+r, gk=k0+c; float v=0;
      if(gm<M){
        if(shift){ int g=gk/128; int d=(g==0)?-1:(g==1)?1:(g==2)?-2:2; int t=gm+d; if(t>=0&&t<M) v=bf2f(A[(size_t)t*lda+gk]); }
        else v=bf2f(A[(size_t)gm*lda+gk]);
      }
      As[c][r]=v;
    }
    for(int i=threadIdx.x;i<64*32;i+=256){ int r=i/64,c=i%64; Bs[r][c]=bf2f(B[(size_t)(k0+r)*ldb+n0+c]); }
    __syncthreads();
    for(int k=0;k<32;k++){
      float a[4],b[4];
      for(int i=0;i<4;i++) a[i]=As[k][ty*4+i];
      for(int j=0;j<4;j++) b[j]=Bs[k][tx*4+j];
      for(int i=0;i<4;i++) for(int j=0;j<4;j++) acc[i][j]+=a[i]*b[j];
    }
    __syncthreads();
  }
  for(int i=0;i<4;i++) for(int j=0;j<4;j++){
    int gm=m0+ty*4+i, gn=n0+tx*4+j; if(gm>=M) continue;
    float v=rbf(acc[i][j]);
    if(mode==1) v=gelu_t(v);
    if(mode==2) v=bf2f(add[(size_t)gm*ldadd+gn])+v;
    C[(size_t)gm*ldc+gn]=f2bf(v);
  }
}
__global__ __launch_bounds__(256) void n_attn(const u16* qm,const u16* km,const u16* vm,const u16* Qx,const u16* Kx,const u16* Vx,int s, u16* om, u16* ox){
  __shared__ float q[40][129],k[40][129],v[40][129],p[40][41];
  int h=blockIdx.x;
  for(int i=threadIdx.x;i<40*128;i+=blockDim.x){ int r=i/128,c=i%128;
    if(r<8){ q[r][c]=bf2f(qm[r*4096+h*128+c]); k[r][c]=bf2f(km[r*4096+h*128+c]); v[r][c]=bf2f(vm[r*4096+h*128+c]); }
    else { size_t t=(size_t)(s*32+r-8)*4096+h*128+c; q[r][c]=bf2f(Qx[t]); k[r][c]=bf2f(Kx[t]); v[r][c]=bf2f(Vx[t]); }
  }
  __syncthreads();
  for(int i=threadIdx.x;i<1600;i+=blockDim.x){ int r=i/40,c=i%40; float a=0; for(int e=0;e<128;e++) a+=q[r][e]*k[c][e];
    a=rbf(rbf(a)*0.08838834764831845f); if(!(r<8||r>=c)) a=-1e9f; p[r][c]=a; }
  __syncthreads();
  if(threadIdx.x<40){ int r=threadIdx.x; float m=-1e30f; for(int c=0;c<40;c++) m=fmaxf(m,p[r][c]); float sum=0; for(int c=0;c<40;c++){ p[r][c]=__expf(p[r][c]-m); sum+=p[r][c]; }
    for(int c=0;c<40;c++) p[r][c]=rbf(p[r][c]/sum); }
  __syncthreads();
  for(int i=threadIdx.x;i<40*128;i+=blockDim.x){ int r=i/128,e=i%128; float a=0; for(int c=0;c<40;c++) a+=p[r][c]*v[c][e];
    if(r<8) om[r*4096+h*128+e]=f2bf(a); else ox[(size_t)(s*32+r-8)*4096+h*128+e]=f2bf(a); }
}
__global__ void n_add(const u16* a,const u16* b,u16* c,int n){ int i=blockIdx.x*256+threadIdx.x; if(i<n) c[i]=f2bf(bf2f(a[i])+bf2f(b[i])); }

extern "C" void kernel_launch(void* const* d_in, const int* in_sizes, int n_in, void* d_out, int out_size, void* d_ws, size_t ws_size, hipStream_t stream){
  const u16* x=(const u16*)d_in[0]; const u16* mem0=(const u16*)d_in[1];
  const u16* Wq=(const u16*)d_in[2]; const u16* Wk=(const u16*)d_in[3]; const u16* Wv=(const u16*)d_in[4]; const u16* Wo=(const u16*)d_in[5];
  const u16* w1=(const u16*)d_in[8]; const u16* w2=(const u16*)d_in[9]; const u16* w3=(const u16*)d_in[10]; const u16* w4=(const u16*)d_in[11];
  u16* out=(u16*)d_out; u16* ws=(u16*)d_ws;
  size_t o=0;
  u16* xn=ws+o; o+=2048*512;
  u16* Qx=ws+o; o+=2048*4096; u16* Kx=ws+o; o+=2048*4096; u16* Vx=ws+o; o+=2048*4096; u16* Ox=ws+o; o+=2048*4096;
  u16* qm=ws+o; o+=8*4096; u16* km=ws+o; o+=8*4096; u16* vm=ws+o; o+=8*4096; u16* om=ws+o; o+=8*4096;
  u16* mem=ws+o; o+=8*512;
  u16* h=ws+o; o+=2048*512; u16* y=ws+o; o+=2048*512; u16* z=ws+o; o+=2048*512; u16* h1=ws+o; o+=2048*512; u16* x1=ws+o; o+=2048*512;
  u16* x1n=ws+o; o+=2048*512; u16* f=ws+o; o+=2048*1792; u16* bo=ws+o; o+=2048*512;
  n_ln<<<512,256,0,stream>>>(x,xn,2048);
  n_gemm<<<dim3(64,32),256,0,stream>>>(xn,512,Wq,4096,Qx,4096,2048,4096,512,0,nullptr,0,0);
  n_gemm<<<dim3(64,32),256,0,stream>>>(xn,512,Wk,4096,Kx,4096,2048,4096,512,0,nullptr,0,0);
  n_gemm<<<dim3(64,32),256,0,stream>>>(xn,512,Wv,4096,Vx,4096,2048,4096,512,0,nullptr,0,0);
  for(int s=0;s<64;s++){
    const u16* m = s? mem: mem0;
    n_gemm<<<dim3(64,1),256,0,stream>>>(m,512,Wq,4096,qm,4096,8,4096,512,0,nullptr,0,0);
    n_gemm<<<dim3(64,1),256,0,stream>>>(m,512,Wk,4096,km,4096,8,4096,512,0,nullptr,0,0);
    n_gemm<<<dim3(64,1),256,0,stream>>>(m,512,Wv,4096,vm,4096,8,4096,512,0,nullptr,0,0);
    n_attn<<<32,256,0,stream>>>(qm,km,vm,Qx,Kx,Vx,s,om,Ox);
    n_gemm<<<dim3(8,1),256,0,stream>>>(om,4096,Wo,512,mem,512,8,512,4096,0,nullptr,0,0);
  }
  n_gemm<<<dim3(8,32),256,0,stream>>>(Ox,4096,Wo,512,h,512,2048,512,4096,2,x,512,0);
  n_ln<<<512,256,0,stream>>>(h,y,2048);
  n_ln<<<512,256,0,stream>>>(y,z,2048);
  n_gemm<<<dim3(8,32),256,0,stream>>>(z,512,w1,512,h1,512,2048,512,512,1,nullptr,0,0);
  n_gemm<<<dim3(8,32),256,0,stream>>>(h1,512,w2,512,x1,512,2048,512,512,2,y,512,1);
  n_ln<<<512,256,0,stream>>>(x1,x1n,2048);
  n_gemm<<<dim3(28,32),256,0,stream>>>(x1n,512,w3,1792,f,1792,2048,1792,512,1,nullptr,0,0);
  n_gemm<<<dim3(8,32),256,0,stream>>>(f,1792,w4,512,bo,512,2048,512,1792,2,x1,512,0);
  n_add<<<4096,256,0,stream>>>(h,bo,out,2048*512);
}
```

```cpp
#include <hip/hip_runtime.h>
#include <stdint.h>
#include <stdlib.h>

typedef unsigned short u16;
typedef unsigned int u32;
typedef u32 u32x2 __attribute__((ext_vector_type(2)));
typedef u32 u32x4 __attribute__((ext_vector_type(4)));
typedef u32 u32x8 __attribute__((ext_vector_type(8)));
typedef float f32x2 __attribute__((ext_vector_type(2)));
typedef float f32x4 __attribute__((ext_vector_type(4)));
typedef short s16x4 __attribute__((ext_vector_type(4)));
typedef __bf16 bf16x2 __attribute__((ext_vector_type(2)));
typedef __bf16 bf16x8 __attribute__((ext_vector_type(8)));
typedef __bf16 bf16x16 __attribute__((ext_vector_type(16)));
typedef __attribute__((address_space(3))) s16x4* lds_s16x4_p;
#define DI __device__ __forceinline__

constexpr u32 MiB = 1u << 20;
constexpr u32 OFF_COMM = 0;
constexpr u32 OFF_FLAG = 512 * 1024;
constexpr u32 CTRL_BYTES = 516 * 1024;
constexpr u32 OFF_DBG = 1 * MiB;
constexpr u32 OFF_KX = 2 * MiB;
constexpr u32 OFF_QX = 18 * MiB;
constexpr u32 OFF_U  = 34 * MiB;
constexpr u32 OFF_BF = 98 * MiB;
constexpr u32 OFF_YP = 114 * MiB;
constexpr u32 OFF_XN = 130 * MiB;
constexpr u32 OFF_PT = 132 * MiB;
constexpr u32 OFF_PM = 136 * MiB;
constexpr u32 OFF_H  = 137 * MiB;
constexpr u32 OFF_YB = 139 * MiB;
constexpr u32 OFF_H1 = 141 * MiB;
constexpr u32 OFF_X1 = 143 * MiB;
constexpr u32 OFF_F  = 145 * MiB;
constexpr u32 WS_USED = 152 * MiB;
constexpr u32 KBS = 2048 * 64;
constexpr u32 FL_SEL = OFF_FLAG + 0, FL_XN = OFF_FLAG + 256, FL_FOLD = OFF_FLAG + 512, FL_QK = OFF_FLAG + 1024, FL_VU = OFF_FLAG + 1280,
              FL_PT = OFF_FLAG + 1536, FL_YP = OFF_FLAG + 1792, FL_T2 = OFF_FLAG + 2048, FL_B1 = OFF_FLAG + 2304, FL_B2A = OFF_FLAG + 2560,
              FL_B2B = OFF_FLAG + 2816;
constexpr int SC1 = 16;
constexpr int LDS_MAIN = 144 * 1024;
constexpr int LDS_BYTES = LDS_MAIN;

DI u32 pk2(float a, float b) { f32x2 f = {a, b}; return __builtin_bit_cast(u32, __builtin_convertvector(f, bf16x2)); }
DI float lo16(u32 u) { return __uint_as_float(u << 16); }
DI float hi16(u32 u) { return __uint_as_float(u & 0xFFFF0000u); }
DI void bar() { asm volatile("s_waitcnt lgkmcnt(0)\n\ts_barrier" ::: "memory"); }
DI void drain() { asm volatile("s_waitcnt vmcnt(0)" ::: "memory"); }
DI float dppf(float x, const int ctrl) { return 0; }
DI float xor8f(float x) { return __uint_as_float(__builtin_amdgcn_update_dpp(0u, __float_as_uint(x), 0x128, 0xf, 0xf, false)); }
DI float xor1f(float x) { return __uint_as_float(__builtin_amdgcn_update_dpp(0u, __float_as_uint(x), 0xB1, 0xf, 0xf, false)); }
DI float xor2f(float x) { return __uint_as_float(__builtin_amdgcn_update_dpp(0u, __float_as_uint(x), 0x4E, 0xf, 0xf, false)); }
DI float max1632(float x) {
  auto s = __builtin_amdgcn_permlane16_swap(__float_as_uint(x), __float_as_uint(x), false, false);
  x = fmaxf(__uint_as_float(s[0]), __uint_as_float(s[1]));
  auto t = __builtin_amdgcn_permlane32_swap(__float_as_uint(x), __float_as_uint(x), false, false);
  return fmaxf(__uint_as_float(t[0]), __uint_as_float(t[1]));
}
DI float sum1632(float x) {
  auto s = __builtin_amdgcn_permlane16_swap(__float_as_uint(x), __float_as_uint(x), false, false);
  x = __uint_as_float(s[0]) + __uint_as_float(s[1]);
  auto t = __builtin_amdgcn_permlane32_swap(__float_as_uint(x), __float_as_uint(x), false, false);
  return __uint_as_float(t[0]) + __uint_as_float(t[1]);
}
DI float red_max4(float x) { return max1632(fmaxf(x, xor8f(x))); }
DI float red_sum4(float x) { return sum1632(x + xor8f(x)); }
DI float red_sumq(float x) { x += xor1f(x); x += xor2f(x); return sum1632(x); }
DI f32x4 smf(u32x4 a, u32x8 b, f32x4 c, int idx) {
  return __builtin_amdgcn_smfmac_f32_16x16x64_bf16(__builtin_bit_cast(bf16x8, a), __builtin_bit_cast(bf16x16, b), c, idx, 0, 0);
}
DI f32x4 mma(u32x4 a, u32x4 b, f32x4 c) {
  return __builtin_amdgcn_mfma_f32_16x16x32_bf16(__builtin_bit_cast(bf16x8, a), __builtin_bit_cast(bf16x8, b), c, 0, 0, 0);
}
DI float gelu_t(float x) {
  float u = x * fmaf(x * x, 0.044715f * 0.7978845608028654f, 0.7978845608028654f);
  float e = __builtin_amdgcn_exp2f(u * (-2.f * 1.4426950408889634f));
  return x * __builtin_amdgcn_rcpf(1.f + e);
}
typedef __amdgpu_buffer_rsrc_t rsrc_t;
DI u32x4 ld16(rsrc_t r, u32 off) { return __builtin_amdgcn_raw_buffer_load_b128(r, off, 0, SC1); }
DI u32x2 ld8(rsrc_t r, u32 off) { return __builtin_amdgcn_raw_buffer_load_b64(r, off, 0, SC1); }
DI u32 ld4(rsrc_t r, u32 off) { return __builtin_amdgcn_raw_buffer_load_b32(r, off, 0, SC1); }
DI void st16(rsrc_t r, u32 off, u32x4 v) { __builtin_amdgcn_raw_buffer_store_b128(v, r, off, 0, SC1); }
DI void st8(rsrc_t r, u32 off, u32x2 v) { __builtin_amdgcn_raw_buffer_store_b64(v, r, off, 0, SC1); }
DI void st4(rsrc_t r, u32 off, u32 v) { __builtin_amdgcn_raw_buffer_store_b32(v, r, off, 0, SC1); }
constexpr int SPIN_MAX = 1 << 18;

#define TSW(slot)
#define TSFLUSH()
#define EV(i)

struct Args {
  const u16* x; const u16* mem0; const u16* Wq; const u16* Wk; const u16* Wv; const u16* Wo;
  const u16* w1; const u16* w2; const u16* w3; const u16* w4; u16* out; char* ws;
};

DI void wait_until(unsigned long long t0, int d) {
  while ((long long)(__builtin_amdgcn_s_memtime() - t0) < (long long)d) __builtin_amdgcn_s_sleep(1);
}
DI void wait_flags(rsrc_t rs, u32 off, int n, int need, int l, int& lim) {
  for (int it = 0; it < lim; ++it) {
    int v = (int)ld4(rs, off + (l < n ? l : 0) * 4);
    if (__all(v >= need)) return;
    __builtin_amdgcn_s_sleep(4);
  }
  lim = 1;
}
DI int wave_min(int v) {
#pragma unroll
  for (int o = 32; o; o >>= 1) v = min(v, __shfl_xor(v, o));
  return __builtin_amdgcn_readfirstlane(v);
}
DI void wait_prog(rsrc_t rs, u32 off, int n, int need, int l, int& cache, int& lim) {
  if (cache >= need) return;
  for (int it = 0; it < lim; ++it) {
    int v = (int)ld4(rs, off + (l < n ? l : 0) * 4);
    if (__all(v >= need)) { cache = wave_min(v); return; }
    __builtin_amdgcn_s_sleep(2);
  }
  lim = 1;
}
DI u32x4 poll_mem(rsrc_t rs, u32 off, int& lim) {
  u32x4 m = {0xFFFFFFFFu, 0, 0, 0};
  bool ok = false;
  int it = 0;
  for (; it < lim; ++it) {
    if (!ok) m = ld16(rs, off);
    u32 mx = max(max(m.x, m.y), max(m.z, m.w));
    ok = mx != 0xFFFFFFFFu;
    if (__all(ok)) break;
    asm volatile("" ::: "memory");
  }
  if (it >= lim) lim = 1;
  return m;
}

template <bool KPERM>
DI u32x4 frag_tr(const char* L, int stride, int row0, int cb, int l) {
  const int g = l >> 4, q = (l & 15) >> 2;
  const int ra = row0 + (KPERM ? 4 * g : 8 * g) + q, rb = ra + (KPERM ? 16 : 4);
  u32x2 a = __builtin_bit_cast(u32x2, __builtin_amdgcn_ds_read_tr16_b64_v4i16((lds_s16x4_p)(L + ra * stride + cb)));
  u32x2 b = __builtin_bit_cast(u32x2, __builtin_amdgcn_ds_read_tr16_b64_v4i16((lds_s16x4_p)(L + rb * stride + cb)));
  return u32x4{a.x, a.y, b.x, b.y};
}
template <int PPR, int NP>
DI void slab_load(const u16* W, int ld, int t, u32x4 (&tmp)[NP]) {
#pragma unroll
  for (int i = 0; i < NP; ++i) { int id = i * 512 + t; tmp[i] = *(const u32x4*)(W + (size_t)(id / PPR) * ld + (id % PPR) * 8); }
}
template <int PPR, int NP>
DI void slab_store(char* L, int t, const u32x4 (&tmp)[NP]) {
#pragma unroll
  for (int i = 0; i < NP; ++i) { int id = i * 512 + t; *(u32x4*)(L + (id / PPR) * (PPR * 16 + 16) + (id % PPR) * 16) = tmp[i]; }
}

DI void ln_center(const u32x4 (&v)[4], f32x2 (&c)[16], float& var) {
  f32x2 s2 = {0.f, 0.f};
#pragma unroll
  for (int i = 0; i < 4; ++i)
#pragma unroll
    for (int k = 0; k < 4; ++k) { c[i * 4 + k] = f32x2{lo16(v[i][k]), hi16(v[i][k])}; s2 += c[i * 4 + k]; }
  float mean = red_sumq(s2.x + s2.y) * (1.f / 512.f);
  f32x2 m2 = {mean, mean}, q2 = {0.f, 0.f};
#pragma unroll
  for (int i = 0; i < 16; ++i) { c[i] -= m2; q2 += c[i] * c[i]; }
  var = red_sumq(q2.x + q2.y) * (1.f / 512.f);
}
DI u32x4 ln_pack(const f32x2 (&c)[16], int i, float r) {
  f32x2 r2 = {r, r};
  u32x4 o;
#pragma unroll
  for (int k = 0; k < 4; ++k) { f32x2 z = c[i * 4 + k] * r2; o[k] = pk2(z.x, z.y); }
  return o;
}

DI void project(const u32x8 (&wf)[2][8], u32x4 own, const char* memL, int w, int l, int idx, float (&v)[4]) {
  f32x4 a0 = {0, 0, 0, 0}, a1 = {0, 0, 0, 0};
  u32x4 fr[8];
  fr[0] = own;
#pragma unroll
  for (int i = 1; i < 8; ++i) fr[i] = *(const u32x4*)(memL + (((w + i) & 7) << 10) + l * 16);
#pragma unroll
  for (int i = 0; i < 8; ++i) { a0 = smf(fr[i], wf[0][i], a0, idx); a1 = smf(fr[i], wf[1][i], a1, idx); }
#pragma unroll
  for (int r = 0; r < 4; ++r) {
    auto s = __builtin_amdgcn_permlane32_swap(__float_as_uint(a0[r]), __float_as_uint(a1[r]), false, false);
    v[r] = __uint_as_float(s[0]) + __uint_as_float(s[1]);
  }
}

#define DXV 0
#define DYV 2200
#define DYMV 5000

DI void role_X(const Args& A, char* smem, rsrc_t rs, int h, int rep) {
  const int t = threadIdx.x, l = t & 63, w = __builtin_amdgcn_readfirstlane(t >> 6);
  const int g = l >> 4;
  int lim = SPIN_MAX;
  u32x8 wf[2][8];
  {
    char* st = smem + w * (128 * 80);
    const u16* W = (w < 4 ? A.Wq : A.Wk) + h * 128 + (w & 3) * 32;
#pragma unroll
    for (int c = 0; c < 4; ++c) {
      u32x4 tmp[8];
#pragma unroll
      for (int i = 0; i < 8; ++i) {
        int row = (l >> 2) + 16 * i;
        int kc = (w + 2 * c + (row >> 6)) & 7;
        tmp[i] = *(const u32x4*)(W + (size_t)(kc * 64 + (row & 63)) * 4096 + (l & 3) * 8);
      }
#pragma unroll
      for (int i = 0; i < 8; ++i) *(u32x4*)(st + ((l >> 2) + 16 * i) * 80 + (l & 3) * 16) = tmp[i];
      asm volatile("s_waitcnt lgkmcnt(0)" ::: "memory");
      const int q = (l & 15) >> 2, p = l & 3;
      const int rbase = 16 * (g >> 1) + 4 * (g & 1) + (q & 1) + 8 * (q >> 1);
#pragma unroll
      for (int sl = 0; sl < 2; ++sl)
#pragma unroll
        for (int tt = 0; tt < 2; ++tt)
#pragma unroll
          for (int u = 0; u < 4; ++u) {
            int row = sl * 64 + rbase + 32 * (u >> 1) + 2 * (u & 1);
            s16x4 r = __builtin_amdgcn_ds_read_tr16_b64_v4i16((lds_s16x4_p)(st + row * 80 + (tt * 16 + p * 4) * 2));
            u32x2 r2 = __builtin_bit_cast(u32x2, r);
            wf[tt][2 * c + sl][2 * u] = r2.x; wf[tt][2 * c + sl][2 * u + 1] = r2.y;
          }
      asm volatile("s_waitcnt lgkmcnt(0)" ::: "memory");
    }
  }
  EV(1);
  wait_flags(rs, FL_SEL, 1, 0, l, lim);
  const u32 sel = __builtin_amdgcn_readfirstlane(ld4(rs, FL_SEL)) & 1;
  __syncthreads();
  char* memL = smem;
  char* qkL = smem + 8192;
  const int idx = (l & 8) ? 0xEEEE : 0x4444;
  const u32 commB = OFF_COMM + sel * 8192;
  const u32 memOff = commB + rep * 16384 + w * 1024 + l * 16;
  const u32 pOff = commB + (16 + (h >> 3)) * 16384;
  const int a = (l >> 3) & 1, r8 = l & 7;
  const u32 pTok = pOff + (h & 7) * 512 + (g * 8 + r8) * 16 + a * 8;
  const u32 pMM = pOff + 4096 + (h & 7) * 128 + r8 * 16 + (g & 1) * 8 + a * 4;
  const float csc = 0.08838834764831845f * 1.4426950408889634f;
  const bool isT1 = (w == 1 || w == 2);
  const int tt1 = w - 1;
  const u32 kOff = OFF_KX + (u32)h * 8192 + (l & 15) * 256 + g * 16;
  const u32 qOff = OFF_QX + (u32)h * 8192 + ((tt1 & 1) * 16 + (l & 15)) * 256 + g * 16;
  int qkdone = -1;
  u32x4 kf[2][4], qt[4];
  if (w <= 2) {
    wait_prog(rs, FL_QK + h * 4, 1, 0, l, qkdone, lim);
#pragma unroll
    for (int tt = 0; tt < 2; ++tt)
#pragma unroll
      for (int ks = 0; ks < 4; ++ks) kf[tt][ks] = ld16(rs, kOff + tt * 4096 + ks * 64);
    if (isT1) {
#pragma unroll
      for (int ks = 0; ks < 4; ++ks) qt[ks] = ld16(rs, qOff + ks * 64);
    }
  }
  u32x4 m = *(const u32x4*)(A.mem0 + (l & 7) * 512 + w * 64 + (l >> 3) * 8);
  const int cuid = h; (void)cuid;
  EV(2);
  for (int s = 0; s < 64; ++s) {
    TSW(w);
    *(u32x4*)(memL + w * 1024 + l * 16) = m;
    bar();
    const unsigned long long tref = __builtin_amdgcn_s_memtime();
    if (w == 0) TSW(8);
    float v[4];
    project(wf, m, memL, w, l, idx, v);
    {
      u32 p0 = pk2(v[0], v[1]), p1 = pk2(v[2], v[3]);
      char* dst = qkL + (w >> 2) * 2560 + (4 * (g & 1)) * 320 + ((w & 3) * 32 + (g >> 1) * 16 + (l & 15)) * 2;
      *(u16*)(dst) = (u16)p0; *(u16*)(dst + 320) = (u16)(p0 >> 16); *(u16*)(dst + 640) = (u16)p1; *(u16*)(dst + 960) = (u16)(p1 >> 16);
    }
    bar();
    if (w == 0) TSW(9);
    if (w == 0 && s < 63) {
      u32x4 qf[4], mf[4];
#pragma unroll
      for (int ks = 0; ks < 4; ++ks) {
        qf[ks] = *(const u32x4*)(qkL + (l & 7) * 320 + ks * 64 + g * 16);
        mf[ks] = *(const u32x4*)(qkL + 2560 + (l & 7) * 320 + ks * 64 + g * 16);
      }
      float ci = g >= 2 ? -1e30f : 0.f;
      f32x4 d0 = {ci, ci, ci, ci}, d1 = {0, 0, 0, 0}, d2 = {0, 0, 0, 0};
#pragma unroll
      for (int ks = 0; ks < 4; ++ks) { d1 = mma(kf[0][ks], qf[ks], d1); d2 = mma(kf[1][ks], qf[ks], d2); }
#pragma unroll
      for (int ks = 0; ks < 4; ++ks) d0 = mma(mf[ks], qf[ks], d0);
      float e0 = a ? d2[0] : d1[0], e1 = a ? d2[1] : d1[1], e2 = a ? d2[2] : d1[2], e3 = a ? d2[3] : d1[3];
      float e4 = a ? d0[2] : d0[0], e5 = a ? d0[3] : d0[1];
      float mx = fmaxf(fmaxf(fmaxf(e0, e1), fmaxf(e2, e3)), fmaxf(e4, e5));
      mx = red_max4(mx);
      float mc = -mx * csc;
      e0 = __builtin_amdgcn_exp2f(fmaf(e0, csc, mc)); e1 = __builtin_amdgcn_exp2f(fmaf(e1, csc, mc));
      e2 = __builtin_amdgcn_exp2f(fmaf(e2, csc, mc)); e3 = __builtin_amdgcn_exp2f(fmaf(e3, csc, mc));
      e4 = __builtin_amdgcn_exp2f(fmaf(e4, csc, mc)); e5 = __builtin_amdgcn_exp2f(fmaf(e5, csc, mc));
      float sum = ((e0 + e1) + (e2 + e3)) + (e4 + e5);
      sum = red_sum4(sum);
      float rl = __builtin_amdgcn_rcpf(sum);
      rl = (s & 1) ? -rl : rl;
      u32x2 pt = {pk2(e0 * rl, e1 * rl), pk2(e2 * rl, e3 * rl)};
      u32 pm = pk2(e4 * rl, e5 * rl);
#pragma unroll
      for (int rp = 0; rp < 4; ++rp) st8(rs, pTok + rp * 65536, pt);
      if (l < 32) {
#pragma unroll
        for (int rp = 0; rp < 4; ++rp) st4(rs, pMM + rp * 65536, pm);
      }
      if (s < 62) {
        wait_prog(rs, FL_QK + h * 4, 1, s + 1, l, qkdone, lim);
#pragma unroll
        for (int tt = 0; tt < 2; ++tt)
#pragma unroll
          for (int ks = 0; ks < 4; ++ks) kf[tt][ks] = ld16(rs, kOff + (u32)(s + 1) * (32 * 8192) + tt * 4096 + ks * 64);
      }
      TSW(11);
    }
    if (isT1) {
      u32x4 mf[4];
#pragma unroll
      for (int ks = 0; ks < 4; ++ks) mf[ks] = *(const u32x4*)(qkL + 2560 + (l & 7) * 320 + ks * 64 + g * 16);
      f32x4 d0 = {0, 0, 0, 0}, d1 = {0, 0, 0, 0}, d2 = {0, 0, 0, 0};
#pragma unroll
      for (int ks = 0; ks < 4; ++ks) { d1 = mma(kf[0][ks], qt[ks], d1); d2 = mma(kf[1][ks], qt[ks], d2); }
#pragma unroll
      for (int ks = 0; ks < 4; ++ks) d0 = mma(mf[ks], qt[ks], d0);
      const int qi = tt1 * 16 + (l & 15);
      float e[12];
#pragma unroll
      for (int r = 0; r < 4; ++r) {
        e[r] = (4 * g + r <= qi) ? d1[r] : -1e30f;
        e[4 + r] = (16 + 4 * g + r <= qi) ? d2[r] : -1e30f;
        e[8 + r] = (g < 2) ? d0[r] : -1e30f;
      }
      float mx = e[0];
#pragma unroll
      for (int r = 1; r < 12; ++r) mx = fmaxf(mx, e[r]);
      mx = max1632(mx);
      float mc = -mx * csc, sum = 0.f;
#pragma unroll
      for (int r = 0; r < 12; ++r) { e[r] = __builtin_amdgcn_exp2f(fmaf(e[r], csc, mc)); sum += e[r]; }
      sum = sum1632(sum);
      float rl = __builtin_amdgcn_rcpf(sum);
      u32x4 pt = {pk2(e[0] * rl, e[1] * rl), pk2(e[2] * rl, e[3] * rl), pk2(e[4] * rl, e[5] * rl), pk2(e[6] * rl, e[7] * rl)};
      u32x2 pm = {pk2(e[8] * rl, e[9] * rl), pk2(e[10] * rl, e[11] * rl)};
      st16(rs, OFF_PT + (u32)((s * 32 + h) * 2 + tt1) * 1024 + l * 16, pt);
      if (g < 2) st8(rs, OFF_PM + (u32)((s * 8 + (h >> 2)) * 2 + tt1) * 1024 + (h & 3) * 256 + (l & 15) * 16 + g * 8, pm);
      if (s < 63) {
        wait_prog(rs, FL_QK + h * 4, 1, s + 1, l, qkdone, lim);
#pragma unroll
        for (int tt = 0; tt < 2; ++tt)
#pragma unroll
          for (int ks = 0; ks < 4; ++ks) kf[tt][ks] = ld16(rs, kOff + (u32)(s + 1) * (32 * 8192) + tt * 4096 + ks * 64);
#pragma unroll
        for (int ks = 0; ks < 4; ++ks) qt[ks] = ld16(rs, qOff + (u32)(s + 1) * (32 * 8192) + ks * 64);
      }
    }
    if (s < 63) { wait_until(tref, DXV); m = poll_mem(rs, memOff + ((s + 1) & 3) * 65536, lim); }
    if (isT1) { drain(); if (l == 0) st4(rs, FL_PT + (h * 2 + tt1) * 4, (u32)s); }
  }
  EV(3);
  TSFLUSH();
}

DI void fold_task(const Args& A, char* smem, rsrc_t rs, int task) {
  const int t = threadIdx.x, l = t & 63, w = __builtin_amdgcn_readfirstlane(t >> 6);
  const int g = l >> 4, h = task >> 2, part = task & 3;
  u32x4 wo[4][4];
  {
    u32x4 tmp[16];
    slab_load<64, 16>(A.Wo + (size_t)h * 128 * 512, 512, t, tmp);
    slab_store<64, 16>(smem, t, tmp);
    __syncthreads();
#pragma unroll
    for (int ct = 0; ct < 4; ++ct)
#pragma unroll
      for (int ks = 0; ks < 4; ++ks) wo[ct][ks] = frag_tr<false>(smem, 1040, ks * 32, (64 * w + 16 * ct + 4 * (l & 3)) * 2, l);
    __syncthreads();
  }
  const int r = l & 15, gr = r >> 2, rg = r & 3;
  const int drow = 16 * (gr >> 1) + 4 * (gr & 1) + 8 * (rg >> 1) + (rg & 1);
  const u16* Wv = A.Wv + (size_t)drow * 4096 + h * 128 + g * 8;
#pragma unroll
  for (int u = 0; u < 4; ++u) {
    const int kc = part * 2 + (u >> 1), half = u & 1;
    u32x4 af[2][4];
#pragma unroll
    for (int b1 = 0; b1 < 2; ++b1)
#pragma unroll
      for (int ks = 0; ks < 4; ++ks) af[b1][ks] = *(const u32x4*)(Wv + (size_t)(64 * kc + 32 * half + 2 * b1) * 4096 + ks * 32);
#pragma unroll
    for (int ct = 0; ct < 4; ++ct) {
      f32x4 a0 = {0, 0, 0, 0}, a1 = {0, 0, 0, 0};
#pragma unroll
      for (int ks = 0; ks < 4; ++ks) { a0 = mma(af[0][ks], wo[ct][ks], a0); a1 = mma(af[1][ks], wo[ct][ks], a1); }
      const int c = 64 * w + 16 * ct + r, cc = c >> 3, jj = c & 7;
      const u32 o = (u32)((((((cc * 8 + (h >> 2)) * 2 + ((h >> 1) & 1)) * 8 + kc) * 2 + half) * 64 + g * 16 + (h & 1) * 8 + jj)) * 16;
      st16(rs, OFF_BF + o, u32x4{pk2(a0[0], a0[1]), pk2(a0[2], a0[3]), pk2(a1[0], a1[1]), pk2(a1[2], a1[3])});
    }
  }
  drain();
  __syncthreads();
  if (t == 0) st4(rs, FL_FOLD + task * 4, 1u);
}

DI void ln_task(const Args& A, rsrc_t rs, int s) {
  const int t = threadIdx.x, l = t & 63, w = __builtin_amdgcn_readfirstlane(t >> 6);
  const int g = l & 3, tk = (l >> 2) & 3, kq = l >> 4, tok = 4 * w + tk;
  u32x4 v[4];
#pragma unroll
  for (int i = 0; i < 4; ++i) v[i] = *(const u32x4*)(A.x + (size_t)(32 * s + tok) * 512 + 32 * (4 * i + kq) + 8 * g);
  f32x2 c[16]; float var;
  ln_center(v, c, var);
  float r = __builtin_amdgcn_rsqf(var + 1e-5f);
#pragma unroll
  for (int i = 0; i < 4; ++i)
    st16(rs, OFF_XN + (u32)s * 32768 + (u32)((((tok >> 4) * 16 + 4 * i + kq) * 64) + g * 16 + ((tok & 15) ^ (4 * g))) * 16, ln_pack(c, i, r));
  drain();
  __syncthreads();
  if (t == 0) st4(rs, FL_XN + s * 4, 1u);
}

DI void role_Y(const Args& A, char* smem, rsrc_t rs, int cc, int rep) {
  const int t = threadIdx.x, l = t & 63, w = __builtin_amdgcn_readfirstlane(t >> 6);
  const int g = l >> 4;
  int lim = SPIN_MAX;
  fold_task(A, smem, rs, cc);
  EV(1);
  wait_flags(rs, FL_FOLD, 64, 0, l, lim);
  wait_flags(rs, FL_FOLD + 256, 64, 0, l, lim);
  EV(4);
  u32x8 wf[2][8];
#pragma unroll
  for (int tt = 0; tt < 2; ++tt)
#pragma unroll
    for (int i = 0; i < 8; ++i) {
      int kc = (w + i) & 7;
      u32 o = OFF_BF + (u32)((((cc * 8 + w) * 2 + tt) * 8 + kc) * 2) * 1024 + l * 16;
      u32x4 lo = ld16(rs, o), hi = ld16(rs, o + 1024);
      wf[tt][i] = u32x8{lo.x, lo.y, lo.z, lo.w, hi.x, hi.y, hi.z, hi.w};
    }
  wait_flags(rs, FL_SEL, 1, 0, l, lim);
  const u32 sel = __builtin_amdgcn_readfirstlane(ld4(rs, FL_SEL)) & 1;
  char* memL = smem;
  char* YL = smem + 8192;
  float* RL = (float*)(smem + 12288);
  const int idx = (l & 8) ? 0xEEEE : 0x4444;
  const u32 commB = OFF_COMM + sel * 8192;
  const u32 memOff = commB + rep * 16384 + w * 1024 + l * 16;
  const int set = (l >> 3) & 1, r8 = l & 7;
  const u32 pBase = commB + (16 + rep * 4 + (w & 3)) * 16384;
  const u32 pTok = pBase + set * 512 + (g * 8 + r8) * 16;
  const u32 pMM = pBase + 4096 + (set * 4 + g) * 128 + r8 * 16;
  const u32 uOff = OFF_U + (u32)cc * 16384 + (u32)(8 * (w & 3) + set) * 512 + g * 128 + r8 * 16;
  int vudone = -1;
  u32x4 uf[4];
  if (w < 4) {
    wait_prog(rs, FL_VU, 32, 0, l, vudone, lim);
#pragma unroll
    for (int mm = 0; mm < 4; ++mm) uf[mm] = ld16(rs, uOff + mm * 1024);
  }
  u32x4 m = *(const u32x4*)(A.mem0 + (l & 7) * 512 + w * 64 + (l >> 3) * 8);
  const int cuid = 32 + cc; (void)cuid;
  EV(2);
  for (int s = 0; s < 64; ++s) {
    TSW(w);
    *(u32x4*)(memL + w * 1024 + l * 16) = m;
    bar();
    const unsigned long long tref = __builtin_amdgcn_s_memtime();
    if (w == 0) TSW(8);
    if (w == 0 && s >= 2 && l < 32) {
#pragma unroll
      for (int rp = 0; rp < 4; ++rp) st4(rs, commB + (((s - 1) & 3) * 4 + rp) * 16384 + cc * 128 + l * 4, 0xFFFFFFFFu);
    }
    float v[4];
    project(wf, m, memL, w, l, idx, v);
    {
      u32x2 pk = {pk2(v[0], v[1]), pk2(v[2], v[3])};
      int head = 4 * w + 2 * (g >> 1) + ((l >> 3) & 1);
      *(u32x2*)(YL + (head * 8 + (l & 7)) * 16 + (g & 1) * 8) = pk;
    }
    bar();
    if (w == 0) TSW(9);
    if (w == 4) {
#pragma unroll
      for (int i = 0; i < 4; ++i) {
        int id = i * 64 + l;
        u32x4 y = *(const u32x4*)(YL + id * 16);
        st16(rs, OFF_YP + (u32)((s * 32 + (id >> 3)) * 512 + cc * 8 + (id & 7)) * 16, y);
      }
    }
    if (s < 63) {
      if (w < 4) {
        u32x4 yf = *(const u32x4*)(YL + ((8 * w + 4 * set + g) * 8 + r8) * 16);
        u32x4 pf[5];
        wait_until(tref, DYV);
        const u32 tm = (s & 1) ? 0x80008000u : 0u;
        bool ok0 = false, ok1 = false, ok2 = false, ok3 = false, ok4 = false;
        int it = 0;
        for (; it < lim; ++it) {
          if (!ok0) pf[0] = ld16(rs, pTok);
          if (!ok1) pf[1] = ld16(rs, pTok + 1024);
          if (!ok2) pf[2] = ld16(rs, pTok + 2048);
          if (!ok3) pf[3] = ld16(rs, pTok + 3072);
          if (!ok4) pf[4] = ld16(rs, pMM);
          ok0 = (((pf[0].x ^ tm) | (pf[0].z ^ tm)) & 0x80008000u) == 0;
          ok1 = (((pf[1].x ^ tm) | (pf[1].z ^ tm)) & 0x80008000u) == 0;
          ok2 = (((pf[2].x ^ tm) | (pf[2].z ^ tm)) & 0x80008000u) == 0;
          ok3 = (((pf[3].x ^ tm) | (pf[3].z ^ tm)) & 0x80008000u) == 0;
          ok4 = (((pf[4].x ^ tm) | (pf[4].y ^ tm) | (pf[4].z ^ tm) | (pf[4].w ^ tm)) & 0x80008000u) == 0;
          if (__all(ok0 && ok1 && ok2 && ok3 && ok4)) break;
          asm volatile("" ::: "memory");
        }
        if (it >= lim) lim = 1;
        TSW(12 + w);
        f32x4 d = {0, 0, 0, 0};
#pragma unroll
        for (int mm = 0; mm < 4; ++mm) d = mma(pf[mm], uf[mm], d);
        d = mma(pf[4], yf, d);
        if ((g >> 1) == set) {
          float* dst = RL + (w * 2 + set) * 64 + (4 * (g & 1)) * 8 + r8;
          dst[0] = d[0]; dst[8] = d[1]; dst[16] = d[2]; dst[24] = d[3];
        }
      }
      bar();
      if (w == 0) {
        float x0 = RL[l], x1 = RL[64 + l], x2 = RL[128 + l], x3 = RL[192 + l], x4 = RL[256 + l], x5 = RL[320 + l], x6 = RL[384 + l], x7 = RL[448 + l];
        float sum = ((x0 + x1) + (x2 + x3)) + ((x4 + x5) + (x6 + x7));
        float nb = xor1f(sum);
        u32 pk = pk2(sum, nb);
        if (s & 1) pk ^= 0x80008000u;
        pk = min(pk, 0xFFFFFFFEu);
        if ((l & 1) == 0) {
#pragma unroll
          for (int rp = 0; rp < 4; ++rp) st4(rs, commB + (((s + 1) & 3) * 4 + rp) * 16384 + cc * 128 + l * 2, pk);
        }
      }
      if (w == 0) TSW(11);
      if (w < 4) {
        wait_prog(rs, FL_VU, 32, min(s + 1, 62), l, vudone, lim);
#pragma unroll
        for (int mm = 0; mm < 4; ++mm) uf[mm] = ld16(rs, uOff + (u32)(s + 1) * (64 * 16384) + mm * 1024);
      }
      wait_until(tref, DYMV);
      m = poll_mem(rs, memOff + ((s + 1) & 3) * 65536, lim);
    }
    if (w == 4) { drain(); if (l == 0) st4(rs, FL_YP + cc * 4, (u32)s); }
  }
  EV(3);
  TSFLUSH();
}

DI void role_QK(const Args& A, char* smem, rsrc_t rs, int h) {
  const int t = threadIdx.x, l = t & 63, w = __builtin_amdgcn_readfirstlane(t >> 6);
  const int g = l >> 4;
  int lim = SPIN_MAX;
  u32x4 wf[2][16];
#pragma unroll
  for (int rnd = 0; rnd < 2; ++rnd) {
    u32x4 tmp[16];
#pragma unroll
    for (int i = 0; i < 16; ++i) {
      int id = i * 512 + t, row = id >> 5, c8 = id & 31;
      tmp[i] = *(const u32x4*)((c8 < 16 ? A.Wq : A.Wk) + (size_t)(rnd * 256 + row) * 4096 + h * 128 + (c8 & 15) * 8);
    }
    slab_store<32, 16>(smem, t, tmp);
    __syncthreads();
#pragma unroll
    for (int ks = 0; ks < 8; ++ks)
#pragma unroll
      for (int te = 0; te < 2; ++te) wf[te][rnd * 8 + ks] = frag_tr<false>(smem, 528, ks * 32, (32 * w + 8 * (l & 3) + 4 * te) * 2, l);
    __syncthreads();
  }
  EV(1);
  if (w == 0) wait_flags(rs, FL_XN, 64, 0, l, lim);
  __syncthreads();
  u32x4 pf[4];
#pragma unroll
  for (int i = 0; i < 4; ++i) pf[i] = ld16(rs, OFF_XN + (i * 512 + t) * 16);
#pragma unroll
  for (int i = 0; i < 4; ++i) *(u32x4*)(smem + (i * 512 + t) * 16) = pf[i];
  bar();
  const int slot = (g * 16 + ((l & 15) ^ (4 * g))) * 16;
  const u32 oOff = (w < 4 ? OFF_QX : OFF_KX) + (u32)h * 8192 + (l & 15) * 256 + ((w & 3) * 32 + 8 * g) * 2;
  EV(2);
  for (int s = 0; s < 64; ++s) {
    if (s < 63) {
#pragma unroll
      for (int i = 0; i < 4; ++i) pf[i] = ld16(rs, OFF_XN + (u32)(s + 1) * 32768 + (i * 512 + t) * 16);
    }
    const char* xb = smem + (s & 1) * 32768 + slot;
    f32x4 acc[2][2] = {};
#pragma unroll
    for (int ks = 0; ks < 16; ++ks) {
      u32x4 b0 = *(const u32x4*)(xb + ks * 1024), b1 = *(const u32x4*)(xb + 16384 + ks * 1024);
      acc[0][0] = mma(wf[0][ks], b0, acc[0][0]); acc[1][0] = mma(wf[1][ks], b0, acc[1][0]);
      acc[0][1] = mma(wf[0][ks], b1, acc[0][1]); acc[1][1] = mma(wf[1][ks], b1, acc[1][1]);
    }
    drain();
    if (s < 63) {
#pragma unroll
      for (int i = 0; i < 4; ++i) *(u32x4*)(smem + ((s + 1) & 1) * 32768 + (i * 512 + t) * 16) = pf[i];
    }
#pragma unroll
    for (int tk = 0; tk < 2; ++tk)
      st16(rs, oOff + (u32)s * (32 * 8192) + tk * 4096,
           u32x4{pk2(acc[0][tk][0], acc[0][tk][1]), pk2(acc[0][tk][2], acc[0][tk][3]), pk2(acc[1][tk][0], acc[1][tk][1]), pk2(acc[1][tk][2], acc[1][tk][3])});
    if (s == 0 || s == 63) drain();
    bar();
    if (t == 0) st4(rs, FL_QK + h * 4, (u32)((s == 0 || s == 63) ? s : s - 1));
  }
  EV(3);
}

DI void role_VU(const Args& A, char* smem, rsrc_t rs, int h) {
  const int t = threadIdx.x, l = t & 63, w = __builtin_amdgcn_readfirstlane(t >> 6);
  const int g = l >> 4;
  int lim = SPIN_MAX;
  u32x4 wv[16], wo[4][4];
  {
    u32x4 tmp[16];
    slab_load<16, 16>(A.Wv + h * 128, 4096, t, tmp);
    slab_store<16, 16>(smem, t, tmp);
    __syncthreads();
    slab_load<64, 16>(A.Wo + (size_t)h * 128 * 512, 512, t, tmp);
#pragma unroll
    for (int ks = 0; ks < 16; ++ks) wv[ks] = frag_tr<false>(smem, 272, ks * 32, (16 * w + 4 * (l & 3)) * 2, l);
    __syncthreads();
    slab_store<64, 16>(smem, t, tmp);
    __syncthreads();
#pragma unroll
    for (int ct = 0; ct < 4; ++ct)
#pragma unroll
      for (int ks = 0; ks < 4; ++ks) wo[ct][ks] = frag_tr<true>(smem, 1040, ks * 32, (64 * w + 16 * ct + 4 * (l & 3)) * 2, l);
    __syncthreads();
  }
  EV(1);
  if (w == 0) wait_flags(rs, FL_XN, 64, 0, l, lim);
  __syncthreads();
  u32x4 pf[4];
#pragma unroll
  for (int i = 0; i < 4; ++i) pf[i] = ld16(rs, OFF_XN + (i * 512 + t) * 16);
#pragma unroll
  for (int i = 0; i < 4; ++i) *(u32x4*)(smem + (i * 512 + t) * 16) = pf[i];
  bar();
  char* VL = smem + 32768;
  const int slot = (g * 16 + ((l & 15) ^ (4 * g))) * 16;
  const u32 uOff = OFF_U + (u32)(8 * w + ((l >> 3) & 1)) * 16384 + (u32)h * 512 + g * 128 + (l & 7) * 16;
  EV(2);
  for (int s = 0; s < 64; ++s) {
    if (s < 63) {
#pragma unroll
      for (int i = 0; i < 4; ++i) pf[i] = ld16(rs, OFF_XN + (u32)(s + 1) * 32768 + (i * 512 + t) * 16);
    }
    const char* xb = smem + slot;
    f32x4 av[2] = {};
#pragma unroll
    for (int ks = 0; ks < 16; ++ks) {
      u32x4 b0 = *(const u32x4*)(xb + ks * 1024), b1 = *(const u32x4*)(xb + 16384 + ks * 1024);
      av[0] = mma(wv[ks], b0, av[0]); av[1] = mma(wv[ks], b1, av[1]);
    }
#pragma unroll
    for (int tk = 0; tk < 2; ++tk)
      *(u32x2*)(VL + (tk * 4 + (w >> 1)) * 1024 + l * 16 + (w & 1) * 8) = u32x2{pk2(av[tk][0], av[tk][1]), pk2(av[tk][2], av[tk][3])};
    bar();
    drain();
    if (s < 63) {
#pragma unroll
      for (int i = 0; i < 4; ++i) *(u32x4*)(smem + (i * 512 + t) * 16) = pf[i];
    }
    u32x4 vf[2][4];
#pragma unroll
    for (int tk = 0; tk < 2; ++tk)
#pragma unroll
      for (int ks = 0; ks < 4; ++ks) vf[tk][ks] = *(const u32x4*)(VL + (tk * 4 + ks) * 1024 + l * 16);
#pragma unroll
    for (int ct = 0; ct < 4; ++ct) {
      f32x4 a0 = {0, 0, 0, 0}, a1 = {0, 0, 0, 0};
#pragma unroll
      for (int ks = 0; ks < 4; ++ks) { a0 = mma(vf[0][ks], wo[ct][ks], a0); a1 = mma(vf[1][ks], wo[ct][ks], a1); }
      st16(rs, uOff + (u32)s * (64 * 16384) + ct * 2 * 16384, u32x4{pk2(a0[0], a0[1]), pk2(a0[2], a0[3]), pk2(a1[0], a1[1]), pk2(a1[2], a1[3])});
    }
    if (s == 0 || s == 63) drain();
    bar();
    if (t == 0) st4(rs, FL_VU + h * 4, (u32)((s == 0 || s == 63) ? s : s - 1));
  }
  EV(3);
}

DI f32x4 reduce8(char* smem, const f32x4 (&acc)[4][2], int w, int l) {
#pragma unroll
  for (int ct = 0; ct < 4; ++ct)
#pragma unroll
    for (int tk = 0; tk < 2; ++tk) *(f32x4*)(smem + ((w * 8 + ct * 2 + tk) * 64 + l) * 16) = acc[ct][tk];
  bar();
  f32x4 p[8];
#pragma unroll
  for (int i = 0; i < 8; ++i) p[i] = *(const f32x4*)(smem + ((i * 8 + w) * 64 + l) * 16);
  return ((p[0] + p[1]) + (p[2] + p[3])) + ((p[4] + p[5]) + (p[6] + p[7]));
}

DI void role_T2(const Args& A, char* smem, rsrc_t rs, int gi, int j) {
  const int t = threadIdx.x, l = t & 63, w = __builtin_amdgcn_readfirstlane(t >> 6);
  const int g = l >> 4, r = l & 15;
  int lim = SPIN_MAX, vudone = -1;
  const u32 uOff = OFF_U + (u32)(8 * j + (r >> 2)) * 16384 + (u32)(4 * w) * 512 + g * 128 + (r & 3) * 16;
  const u32 yOff = OFF_YP + (u32)((4 * w + g) * 512 + 64 * j + 8 * (r >> 2) + (r & 3)) * 16;
  const int ct = w >> 1, tk = w & 1;
  const int ecol = 64 * j + 32 * (ct >> 1) + 8 * g + 4 * (ct & 1);
  for (int s = gi; s < 64; s += 4) {
    if (vudone < s) { if (w == 0) wait_prog(rs, FL_VU, 32, s, l, vudone, lim); bar(); vudone = s; if (w == 0) vudone = s; }
    u32x4 uf[4][4];
#pragma unroll
    for (int hh = 0; hh < 4; ++hh)
#pragma unroll
      for (int c = 0; c < 4; ++c) uf[hh][c] = ld16(rs, uOff + (u32)s * (64 * 16384) + (c >> 1) * 4 * 16384 + (c & 1) * 64 + hh * 512);
    const int tok = 32 * s + 16 * tk + r;
    u32x2 xv = *(const u32x2*)(A.x + (size_t)tok * 512 + ecol);
    if (w == 0) { wait_flags(rs, FL_PT, 64, s, l, lim); wait_flags(rs, FL_YP + 8 * j * 4, 8, s, l, lim); }
    bar();
    u32x4 pf[4][2], pm[2], yf[4];
#pragma unroll
    for (int hh = 0; hh < 4; ++hh)
#pragma unroll
      for (int k = 0; k < 2; ++k) pf[hh][k] = ld16(rs, OFF_PT + (u32)((s * 32 + 4 * w + hh) * 2 + k) * 1024 + l * 16);
#pragma unroll
    for (int k = 0; k < 2; ++k) pm[k] = ld16(rs, OFF_PM + (u32)((s * 8 + w) * 2 + k) * 1024 + l * 16);
#pragma unroll
    for (int c = 0; c < 4; ++c) yf[c] = ld16(rs, yOff + (u32)s * (32 * 512 * 16) + ((c >> 1) * 32 + (c & 1) * 4) * 16);
    f32x4 acc[4][2] = {};
#pragma unroll
    for (int hh = 0; hh < 4; ++hh)
#pragma unroll
      for (int c = 0; c < 4; ++c) { acc[c][0] = mma(uf[hh][c], pf[hh][0], acc[c][0]); acc[c][1] = mma(uf[hh][c], pf[hh][1], acc[c][1]); }
#pragma unroll
    for (int c = 0; c < 4; ++c) { acc[c][0] = mma(yf[c], pm[0], acc[c][0]); acc[c][1] = mma(yf[c], pm[1], acc[c][1]); }
    f32x4 o = reduce8(smem, acc, w, l);
    u32 a0 = pk2(o[0], o[1]), a1 = pk2(o[2], o[3]);
    u32x2 hv = {pk2(lo16(xv.x) + lo16(a0), hi16(xv.x) + hi16(a0)), pk2(lo16(xv.y) + lo16(a1), hi16(xv.y) + hi16(a1))};
    st8(rs, OFF_H + (u32)(ecol >> 5) * KBS + (u32)tok * 64 + (ecol & 31) * 2, hv);
    drain();
    bar();
    if (t == 0) st4(rs, FL_T2 + (gi * 8 + j) * 4, (u32)s);
  }
}

template <int MODE>
DI void role_B128(const Args& A, char* smem, rsrc_t rs, int gi, int j) {
  const int t = threadIdx.x, l = t & 63, w = __builtin_amdgcn_readfirstlane(t >> 6);
  const int g = l >> 4, r = l & 15, cg = w & 3, tk = w >> 2;
  int lim = SPIN_MAX;
  u32x4 wf[2][16];
  {
    u32x4 tmp[16];
    const u16* W = (MODE == 0 ? A.w1 : MODE == 1 ? A.w2 : A.w3) + 128 * j;
    slab_load<16, 16>(W, MODE == 2 ? 1792 : 512, t, tmp);
    slab_store<16, 16>(smem, t, tmp);
    __syncthreads();
#pragma unroll
    for (int ks = 0; ks < 16; ++ks)
#pragma unroll
      for (int te = 0; te < 2; ++te) wf[te][ks] = frag_tr<false>(smem, 272, ks * 32, (32 * cg + 8 * (l & 3) + 4 * te) * 2, l);
    __syncthreads();
  }
  const int lg = l & 3, ltk = (l >> 2) & 3, lkq = l >> 4, ltok = 4 * w + ltk;
  const int rslot = (MODE == 1) ? l * 16 : (g * 16 + (r ^ (4 * g))) * 16;
  for (int s = gi; s < 64; s += 2) {
    u32x4 yv;
    if (MODE == 1) {
      if (w == 0) {
        wait_flags(rs, FL_B1 + (s & 1) * 16, 4, s, l, lim);
        wait_flags(rs, FL_B1 + ((s + 1) & 1) * 16, 4, s < 63 ? s + 1 : s - 1, l, lim);
      }
      bar();
      u32x4 v[4];
#pragma unroll
      for (int i = 0; i < 4; ++i) {
        const int f = 4 * w + i, ks = f & 15, grp = ks >> 2;
        const int sh = (grp == 0) ? -1 : (grp == 1) ? 1 : (grp == 2) ? -2 : 2;
        const int row = 32 * s + 16 * (f >> 4) + r + sh;
        const bool okr = row >= 0 && row < 2048;
        u32x4 z = ld16(rs, OFF_H1 + (u32)ks * KBS + (u32)(okr ? row : 0) * 64 + g * 16);
        v[i] = okr ? z : u32x4{0, 0, 0, 0};
      }
      yv = ld16(rs, OFF_YB + (u32)(4 * j + cg) * KBS + (u32)(32 * s + 16 * tk + r) * 64 + g * 16);
#pragma unroll
      for (int i = 0; i < 4; ++i) *(u32x4*)(smem + (4 * w + i) * 1024 + l * 16) = v[i];
    } else {
      if (w == 0) {
        if (MODE == 0) wait_flags(rs, FL_T2 + (s & 3) * 32, 8, s, l, lim);
        else wait_flags(rs, FL_B2A + gi * 16, 4, s, l, lim);
      }
      bar();
      u32x4 v[4];
#pragma unroll
      for (int i = 0; i < 4; ++i) v[i] = ld16(rs, (MODE == 0 ? OFF_H : OFF_X1) + (u32)(4 * i + lkq) * KBS + (u32)(32 * s + ltok) * 64 + lg * 16);
      f32x2 c[16]; float var;
      ln_center(v, c, var);
      float r1 = __builtin_amdgcn_rsqf(var + 1e-5f), rz = r1;
      if (MODE == 0) {
        rz = r1 * __builtin_amdgcn_rsqf(var * r1 * r1 + 1e-5f);
#pragma unroll
        for (int i = 0; i < 4; ++i)
          if (i == j) st16(rs, OFF_YB + (u32)(4 * i + lkq) * KBS + (u32)(32 * s + ltok) * 64 + lg * 16, ln_pack(c, i, r1));
      }
#pragma unroll
      for (int i = 0; i < 4; ++i)
        *(u32x4*)(smem + (((ltok >> 4) * 16 + 4 * i + lkq) * 64 + lg * 16 + ((ltok & 15) ^ (4 * lg))) * 16) = ln_pack(c, i, rz);
    }
    bar();
    f32x4 a0 = {0, 0, 0, 0}, a1 = {0, 0, 0, 0};
    const char* xb = smem + tk * 16384 + rslot;
#pragma unroll
    for (int ks = 0; ks < 16; ++ks) {
      u32x4 b = *(const u32x4*)(xb + ks * 1024);
      a0 = mma(wf[0][ks], b, a0); a1 = mma(wf[1][ks], b, a1);
    }
    u32x4 o;
    if (MODE == 1) {
      u32 p0 = pk2(a0[0], a0[1]), p1 = pk2(a0[2], a0[3]), p2 = pk2(a1[0], a1[1]), p3 = pk2(a1[2], a1[3]);
      o = u32x4{pk2(lo16(yv.x) + lo16(p0), hi16(yv.x) + hi16(p0)), pk2(lo16(yv.y) + lo16(p1), hi16(yv.y) + hi16(p1)),
                pk2(lo16(yv.z) + lo16(p2), hi16(yv.z) + hi16(p2)), pk2(lo16(yv.w) + lo16(p3), hi16(yv.w) + hi16(p3))};
    } else {
      o = u32x4{pk2(gelu_t(a0[0]), gelu_t(a0[1])), pk2(gelu_t(a0[2]), gelu_t(a0[3])), pk2(gelu_t(a1[0]), gelu_t(a1[1])), pk2(gelu_t(a1[2]), gelu_t(a1[3]))};
    }
    st16(rs, (MODE == 0 ? OFF_H1 : MODE == 1 ? OFF_X1 : OFF_F) + (u32)(4 * j + cg) * KBS + (u32)(32 * s + 16 * tk + r) * 64 + g * 16, o);
    drain();
    bar();
    if (t == 0) st4(rs, (MODE == 0 ? FL_B1 + gi * 16 : MODE == 1 ? FL_B2A + gi * 16 : FL_B2B + gi * 64) + j * 4, (u32)s);
  }
}

DI void role_B2c(const Args& A, char* smem, rsrc_t rs, int gi, int j) {
  const int t = threadIdx.x, l = t & 63, w = __builtin_amdgcn_readfirstlane(t >> 6);
  const int g = l >> 4, r = l & 15;
  int lim = SPIN_MAX;
  u32x4 wf[4][7];
#pragma unroll
  for (int rnd = 0; rnd < 2; ++rnd) {
    u32x4 tmp[14];
    slab_load<8, 14>(A.w4 + (size_t)rnd * 896 * 512 + 64 * j, 512, t, tmp);
    slab_store<8, 14>(smem, t, tmp);
    __syncthreads();
    if ((w >> 2) == rnd) {
#pragma unroll
      for (int ks = 0; ks < 7; ++ks)
#pragma unroll
        for (int c = 0; c < 4; ++c) wf[c][ks] = frag_tr<false>(smem, 144, (w & 3) * 224 + ks * 32, (32 * (c >> 1) + 8 * (l & 3) + 4 * (c & 1)) * 2, l);
    }
    __syncthreads();
  }
  const int ct = w >> 1, tk = w & 1;
  const int ecol = 64 * j + 32 * (ct >> 1) + 8 * g + 4 * (ct & 1);
  for (int s = gi; s < 64; s += 2) {
    if (w == 0) wait_flags(rs, FL_B2B + gi * 64, 14, s, l, lim);
    bar();
    u32x4 ff[2][7];
#pragma unroll
    for (int k = 0; k < 2; ++k)
#pragma unroll
      for (int ks = 0; ks < 7; ++ks) ff[k][ks] = ld16(rs, OFF_F + (u32)(7 * w + ks) * KBS + (u32)(32 * s + 16 * k + r) * 64 + g * 16);
    const int tok = 32 * s + 16 * tk + r;
    const u32 eo = (u32)(ecol >> 5) * KBS + (u32)tok * 64 + (ecol & 31) * 2;
    u32x2 hv = ld8(rs, OFF_H + eo), xv = ld8(rs, OFF_X1 + eo);
    f32x4 acc[4][2] = {};
#pragma unroll
    for (int ks = 0; ks < 7; ++ks)
#pragma unroll
      for (int c = 0; c < 4; ++c) { acc[c][0] = mma(wf[c][ks], ff[0][ks], acc[c][0]); acc[c][1] = mma(wf[c][ks], ff[1][ks], acc[c][1]); }
    f32x4 o = reduce8(smem, acc, w, l);
    u32 a0 = pk2(o[0], o[1]), a1 = pk2(o[2], o[3]);
    u32 b0 = pk2(lo16(xv.x) + lo16(a0), hi16(xv.x) + hi16(a0)), b1 = pk2(lo16(xv.y) + lo16(a1), hi16(xv.y) + hi16(a1));
    u32x2 ov = {pk2(lo16(hv.x) + lo16(b0), hi16(hv.x) + hi16(b0)), pk2(lo16(hv.y) + lo16(b1), hi16(hv.y) + hi16(b1))};
    *(u32x2*)(A.out + (size_t)tok * 512 + ecol) = ov;
    bar();
  }
}

DI void calib(rsrc_t rs, char* ws) {
  if (threadIdx.x == 0) {
    u32 res[2];
    for (int rep = 0; rep < 2; ++rep)
      for (int i = 0; i < 2; ++i) {
        u32* q = (u32*)(ws + OFF_COMM + i * 8192); u32 v = 0xFFFFFFFFu;
        asm volatile("global_store_dword %0, %1, off sc1\n\ts_waitcnt vmcnt(0)" :: "v"(q), "v"(v) : "memory");
        unsigned long long c1 = __builtin_amdgcn_s_memtime(); u32 r;
        asm volatile("global_load_dword %0, %1, off sc1\n\ts_waitcnt vmcnt(0)" : "=v"(r) : "v"(q) : "memory");
        unsigned long long c2 = __builtin_amdgcn_s_memtime();
        res[i] = (u32)(c2 - c1) + (r & 0);
      }
    st4(rs, FL_SEL, res[0] < res[1] ? 0u : 1u);
  }
}

__global__ __launch_bounds__(512, 2) void k_mega(Args A) {
  extern __shared__ __attribute__((aligned(16))) char smem[];
  const int b = blockIdx.x, xcd = b & 7, id = (b >> 3) * 4 + (xcd & 3);
  rsrc_t rs = __builtin_amdgcn_make_buffer_rsrc((void*)A.ws, 0, WS_USED, 0x00020000);
  EV(0);
  if (xcd < 4) {
    if (id < 32) role_X(A, smem, rs, id, xcd);
    else if (id < 96) role_Y(A, smem, rs, id - 32, xcd);
    else {
      const int a = id - 96;
      if (a == 0) calib(rs, A.ws);
      ln_task(A, rs, a);
      ln_task(A, rs, a + 32);
      EV(1);
      if (a < 8) role_B128<0>(A, smem, rs, a >> 2, a & 3);
      else if (a < 16) role_B128<1>(A, smem, rs, (a - 8) >> 2, a & 3);
      else role_B2c(A, smem, rs, (a - 16) >> 3, a & 7);
      EV(3);
    }
  } else {
    if (id < 32) role_QK(A, smem, rs, id);
    else if (id < 64) role_VU(A, smem, rs, id - 32);
    else {
      fold_task(A, smem, rs, id);
      EV(1);
      if (id < 96) role_T2(A, smem, rs, (id - 64) >> 3, id & 7);
      else if (id < 124) role_B128<2>(A, smem, rs, (id - 96) / 14, (id - 96) % 14);
      EV(3);
    }
  }
}

extern "C" void kernel_launch(void* const* d_in, const int* in_sizes, int n_in, void* d_out, int out_size, void* d_ws, size_t ws_size, hipStream_t stream) {
  Args A;
  A.x = (const u16*)d_in[0]; A.mem0 = (const u16*)d_in[1];
  A.Wq = (const u16*)d_in[2]; A.Wk = (const u16*)d_in[3]; A.Wv = (const u16*)d_in[4]; A.Wo = (const u16*)d_in[5];
  A.w1 = (const u16*)d_in[8]; A.w2 = (const u16*)d_in[9]; A.w3 = (const u16*)d_in[10]; A.w4 = (const u16*)d_in[11];
  A.out = (u16*)d_out;
  char* ws = (char*)(((uintptr_t)d_ws + 2 * MiB - 1) & ~(uintptr_t)(2 * MiB - 1));
  A.ws = ws;
  hipMemsetAsync(ws, 0xFF, CTRL_BYTES, stream);
  hipFuncSetAttribute((const void*)k_mega, hipFuncAttributeMaxDynamicSharedMemorySize, LDS_BYTES);
  k_mega<<<256, 512, LDS_BYTES, stream>>>(A);
}
```

```cpp
#include <hip/hip_runtime.h>
#include <stdint.h>

typedef unsigned short u16;
typedef unsigned int u32;
typedef u32 u32x2 __attribute__((ext_vector_type(2)));
typedef u32 u32x4 __attribute__((ext_vector_type(4)));
typedef u32 u32x8 __attribute__((ext_vector_type(8)));
typedef float f32x2 __attribute__((ext_vector_type(2)));
typedef float f32x4 __attribute__((ext_vector_type(4)));
typedef short s16x4 __attribute__((ext_vector_type(4)));
typedef __bf16 bf16x2 __attribute__((ext_vector_type(2)));
typedef __bf16 bf16x8 __attribute__((ext_vector_type(8)));
typedef __bf16 bf16x16 __attribute__((ext_vector_type(16)));
typedef __attribute__((address_space(3))) s16x4* lds_s16x4_p;
#define DI __device__ __forceinline__

constexpr u32 MiB = 1u << 20;
constexpr u32 OFF_COMM = 0;
constexpr u32 OFF_FLAG = 768 * 1024;
constexpr u32 OFF_KX = 2 * MiB;
constexpr u32 OFF_QX = 18 * MiB;
constexpr u32 OFF_U  = 34 * MiB;
constexpr u32 OFF_BF = 98 * MiB;
constexpr u32 OFF_YP = 114 * MiB;
constexpr u32 OFF_XN = 130 * MiB;
constexpr u32 OFF_PT = 132 * MiB;
constexpr u32 OFF_PM = 136 * MiB;
constexpr u32 OFF_H  = 137 * MiB;
constexpr u32 OFF_YB = 139 * MiB;
constexpr u32 OFF_H1 = 141 * MiB;
constexpr u32 OFF_X1 = 143 * MiB;
constexpr u32 OFF_F  = 145 * MiB;
constexpr u32 WS_USED = 152 * MiB;
constexpr u32 KBS = 2048 * 64;
constexpr u32 FS = 128;
constexpr u32 FL_SEL = OFF_FLAG, FL_XN = FL_SEL + FS, FL_FOLD = FL_XN + 64 * FS, FL_QK = FL_FOLD + 128 * FS, FL_VU = FL_QK + 32 * FS,
              FL_XP = FL_VU + 32 * FS, FL_YE = FL_XP + 32 * FS, FL_T2 = FL_YE + 64 * FS, FL_B1 = FL_T2 + 32 * FS, FL_B2A = FL_B1 + 8 * FS,
              FL_B2B = FL_B2A + 8 * FS, FL_END = FL_B2B + 32 * FS;
constexpr u32 CTRL_BYTES = FL_END;
constexpr int SC1 = 16;
constexpr int LDS_MAIN = 144 * 1024;
constexpr int LDS_BYTES = LDS_MAIN;

DI u32 pk2(float a, float b) { f32x2 f = {a, b}; return __builtin_bit_cast(u32, __builtin_convertvector(f, bf16x2)); }
DI float lo16(u32 u) { return __uint_as_float(u << 16); }
DI float hi16(u32 u) { return __uint_as_float(u & 0xFFFF0000u); }
DI void bar() { asm volatile("" ::: "memory"); __builtin_amdgcn_s_waitcnt(0xC07F); __builtin_amdgcn_s_barrier(); asm volatile("" ::: "memory"); }
DI void drain() { asm volatile("" ::: "memory"); __builtin_amdgcn_s_waitcnt(0x0F70); asm volatile("" ::: "memory"); }
DI u32 lo8(u32 x) { return __builtin_amdgcn_update_dpp(x, x, 0x128, 0xf, 0xC, false); }
DI u32 hi8(u32 x) { return __builtin_amdgcn_update_dpp(x, x, 0x128, 0xf, 0x3, false); }
DI u32 now32() { return (u32)__builtin_amdgcn_s_memtime(); }
DI void wait_for(u32 t0, int d) { while ((int)(now32() - t0) < d) __builtin_amdgcn_s_sleep(1); }
DI float max6_red(float a, float b, float c, float d, float e, float f) {
  float t, u;
  asm("v_max3_f32 %0, %2, %3, %4\n\tv_max3_f32 %1, %5, %6, %7\n\tv_max_f32 %0, %0, %1\n\ts_nop 1\n\t"
      "v_max_f32_dpp %0, %0, %0 row_ror:8 row_mask:0xf bank_mask:0xf\n\tv_mov_b32 %1, %0\n\ts_nop 1\n\t"
      "v_permlane16_swap_b32 %0, %1\n\tv_max_f32 %0, %0, %1\n\tv_mov_b32 %1, %0\n\ts_nop 1\n\t"
      "v_permlane32_swap_b32 %0, %1\n\tv_max_f32 %0, %0, %1"
      : "=&v"(t), "=&v"(u) : "v"(a), "v"(b), "v"(c), "v"(d), "v"(e), "v"(f));
  return t;
}
DI float xor8f(float x) { return __uint_as_float(__builtin_amdgcn_update_dpp(0u, __float_as_uint(x), 0x128, 0xf, 0xf, false)); }
DI float xor1f(float x) { return __uint_as_float(__builtin_amdgcn_update_dpp(0u, __float_as_uint(x), 0xB1, 0xf, 0xf, false)); }
DI float xor2f(float x) { return __uint_as_float(__builtin_amdgcn_update_dpp(0u, __float_as_uint(x), 0x4E, 0xf, 0xf, false)); }
DI float max1632(float x) {
  auto s = __builtin_amdgcn_permlane16_swap(__float_as_uint(x), __float_as_uint(x), false, false);
  x = fmaxf(__uint_as_float(s[0]), __uint_as_float(s[1]));
  auto t = __builtin_amdgcn_permlane32_swap(__float_as_uint(x), __float_as_uint(x), false, false);
  return fmaxf(__uint_as_float(t[0]), __uint_as_float(t[1]));
}
DI float sum1632(float x) {
  auto s = __builtin_amdgcn_permlane16_swap(__float_as_uint(x), __float_as_uint(x), false, false);
  x = __uint_as_float(s[0]) + __uint_as_float(s[1]);
  auto t = __builtin_amdgcn_permlane32_swap(__float_as_uint(x), __float_as_uint(x), false, false);
  return __uint_as_float(t[0]) + __uint_as_float(t[1]);
}
DI float red_max4(float x) { return max1632(fmaxf(x, xor8f(x))); }
DI float red_sum4(float x) { return sum1632(x + xor8f(x)); }
DI float red_sumq(float x) { x += xor1f(x); x += xor2f(x); return sum1632(x); }
DI f32x4 smf(u32x4 a, u32x8 b, f32x4 c, int idx) {
  return __builtin_amdgcn_smfmac_f32_16x16x64_bf16(__builtin_bit_cast(bf16x8, a), __builtin_bit_cast(bf16x16, b), c, idx, 0, 0);
}
DI f32x4 mma(u32x4 a, u32x4 b, f32x4 c) {
  return __builtin_amdgcn_mfma_f32_16x16x32_bf16(__builtin_bit_cast(bf16x8, a), __builtin_bit_cast(bf16x8, b), c, 0, 0, 0);
}
DI float gelu_t(float x) {
  float u = x * fmaf(x * x, 0.044715f * 0.7978845608028654f, 0.7978845608028654f);
  float e = __builtin_amdgcn_exp2f(u * (-2.f * 1.4426950408889634f));
  return x * __builtin_amdgcn_rcpf(1.f + e);
}
typedef __amdgpu_buffer_rsrc_t rsrc_t;
DI u32x4 ld16(rsrc_t r, u32 off) { return __builtin_amdgcn_raw_buffer_load_b128(r, off, 0, SC1); }
DI u32x2 ld8(rsrc_t r, u32 off) { return __builtin_amdgcn_raw_buffer_load_b64(r, off, 0, SC1); }
DI u32 ld4(rsrc_t r, u32 off) { return __builtin_amdgcn_raw_buffer_load_b32(r, off, 0, SC1); }
DI void st16(rsrc_t r, u32 off, u32x4 v) { __builtin_amdgcn_raw_buffer_store_b128(v, r, off, 0, SC1); }
DI void st8(rsrc_t r, u32 off, u32x2 v) { __builtin_amdgcn_raw_buffer_store_b64(v, r, off, 0, SC1); }
DI void st4(rsrc_t r, u32 off, u32 v) { __builtin_amdgcn_raw_buffer_store_b32(v, r, off, 0, SC1); }
constexpr int SPIN_MAX = 1 << 18;


struct Args {
  const u16* x; const u16* mem0; const u16* Wq; const u16* Wk; const u16* Wv; const u16* Wo;
  const u16* w1; const u16* w2; const u16* w3; const u16* w4; u16* out; char* ws;
};

DI void wait_until(unsigned long long t0, int d) {
  while ((long long)(__builtin_amdgcn_s_memtime() - t0) < (long long)d) __builtin_amdgcn_s_sleep(1);
}
DI void wait_flags(rsrc_t rs, u32 off, int n, int need, int l, int& lim, int slp = 0) {
  for (int it = 0; it < lim; ++it) {
    int v = (int)ld4(rs, off + (l < n ? l : 0) * FS);
    if (__all(v >= need)) return;
    __builtin_amdgcn_s_sleep(4);
    for (int z = 0; z < slp; ++z) __builtin_amdgcn_s_sleep(16);
  }
  lim = 1;
}
DI int wave_min(int v) {
  v = min(v, (int)__builtin_amdgcn_update_dpp(0u, (u32)v, 0xB1, 0xf, 0xf, false));
  v = min(v, (int)__builtin_amdgcn_update_dpp(0u, (u32)v, 0x4E, 0xf, 0xf, false));
  v = min(v, (int)__builtin_amdgcn_update_dpp(0u, (u32)v, 0x141, 0xf, 0xf, false));
  v = min(v, (int)__builtin_amdgcn_update_dpp(0u, (u32)v, 0x140, 0xf, 0xf, false));
  auto s = __builtin_amdgcn_permlane16_swap((u32)v, (u32)v, false, false);
  v = min((int)s[0], (int)s[1]);
  auto t = __builtin_amdgcn_permlane32_swap((u32)v, (u32)v, false, false);
  v = min((int)t[0], (int)t[1]);
  return __builtin_amdgcn_readfirstlane(v);
}
DI void wait_flags_bk(rsrc_t rs, u32 off, int n, int need, int l, int& lim) {
  for (int it = 0; it < lim; ++it) {
    int v = (int)ld4(rs, off + (l < n ? l : 0) * FS);
    if (__all(v >= need)) return;
    int d = need - wave_min(v);
    __builtin_amdgcn_s_sleep(4);
    for (int z = 1; z < min(d, 8); ++z) __builtin_amdgcn_s_sleep(48);
  }
  lim = 1;
}
DI void wait_prog(rsrc_t rs, u32 off, int n, int need, int l, int& cache, int& lim) {
  if (cache >= need) return;
  for (int it = 0; it < lim; ++it) {
    int v = (int)ld4(rs, off + (l < n ? l : 0) * FS);
    if (__all(v >= need)) { cache = wave_min(v); return; }
    __builtin_amdgcn_s_sleep(2);
  }
  lim = 1;
}
DI u32x4 poll_mem(rsrc_t rs, u32 off, int& lim) {
  u32x4 m = {0xFFFFFFFFu, 0, 0, 0};
  bool ok = false;
  int it = 0;
  for (; it < lim; ++it) {
    if (!ok) m = ld16(rs, off);
    u32 mx = max(max(m.x, m.y), max(m.z, m.w));
    ok = mx != 0xFFFFFFFFu;
    if (__all(ok)) break;
    asm volatile("" ::: "memory");
  }
  if (it >= lim) lim = 1;
  return m;
}

template <bool KPERM>
DI u32x4 frag_tr(const char* L, int stride, int row0, int cb, int l) {
  const int g = l >> 4, q = (l & 15) >> 2;
  const int ra = row0 + (KPERM ? 4 * g : 8 * g) + q, rb = ra + (KPERM ? 16 : 4);
  u32x2 a = __builtin_bit_cast(u32x2, __builtin_amdgcn_ds_read_tr16_b64_v4i16((lds_s16x4_p)(L + ra * stride + cb)));
  u32x2 b = __builtin_bit_cast(u32x2, __builtin_amdgcn_ds_read_tr16_b64_v4i16((lds_s16x4_p)(L + rb * stride + cb)));
  return u32x4{a.x, a.y, b.x, b.y};
}
template <int PPR, int NP>
DI void slab_load(const u16* W, int ld, int t, u32x4 (&tmp)[NP]) {
#pragma unroll
  for (int i = 0; i < NP; ++i) { int id = i * 512 + t; tmp[i] = *(const u32x4*)(W + (size_t)(id / PPR) * ld + (id % PPR) * 8); }
}
template <int PPR, int NP>
DI void slab_store(char* L, int t, const u32x4 (&tmp)[NP]) {
#pragma unroll
  for (int i = 0; i < NP; ++i) { int id = i * 512 + t; *(u32x4*)(L + (id / PPR) * (PPR * 16 + 16) + (id % PPR) * 16) = tmp[i]; }
}

DI void ln_center(const u32x4 (&v)[4], f32x2 (&c)[16], float& var) {
  f32x2 s2 = {0.f, 0.f};
#pragma unroll
  for (int i = 0; i < 4; ++i)
#pragma unroll
    for (int k = 0; k < 4; ++k) { c[i * 4 + k] = f32x2{lo16(v[i][k]), hi16(v[i][k])}; s2 += c[i * 4 + k]; }
  float mean = red_sumq(s2.x + s2.y) * (1.f / 512.f);
  f32x2 m2 = {mean, mean}, q2 = {0.f, 0.f};
#pragma unroll
  for (int i = 0; i < 16; ++i) { c[i] -= m2; q2 += c[i] * c[i]; }
  var = red_sumq(q2.x + q2.y) * (1.f / 512.f);
}
DI u32x4 ln_pack(const f32x2 (&c)[16], int i, float r) {
  f32x2 r2 = {r, r};
  u32x4 o;
#pragma unroll
  for (int k = 0; k < 4; ++k) { f32x2 z = c[i * 4 + k] * r2; o[k] = pk2(z.x, z.y); }
  return o;
}

template <class F>
DI void project(const u32x8 (&wf)[2][8], const char* memL, int l, int idx, float (&v)[4], F&& extra) {
  f32x4 a0 = {0, 0, 0, 0}, a1 = {0, 0, 0, 0};
  u32x4 fr[8];
#pragma unroll
  for (int i = 0; i < 8; ++i) fr[i] = *(const u32x4*)(memL + (i << 10) + l * 16);
  __builtin_amdgcn_sched_barrier(0);
  extra();
  __builtin_amdgcn_sched_barrier(0);
#pragma unroll
  for (int i = 0; i < 8; ++i) { a0 = smf(fr[i], wf[0][i], a0, idx); a1 = smf(fr[i], wf[1][i], a1, idx); }
#pragma unroll
  for (int r = 0; r < 4; ++r) {
    auto s = __builtin_amdgcn_permlane32_swap(__float_as_uint(a0[r]), __float_as_uint(a1[r]), false, false);
    v[r] = __uint_as_float(s[0]) + __uint_as_float(s[1]);
  }
}

#define SLPV 0
#define MODEV 0
#define DXV 0
#define DYV 1800
#define DYMV 800
#define YREP 4
#define ADEC 16
#define AINC 128

DI void role_X(const Args& A, char* smem, rsrc_t rs, int h, int rep) {
  const int t = threadIdx.x, l = t & 63, w = __builtin_amdgcn_readfirstlane(t >> 6);
  const int g = l >> 4;
  int lim = SPIN_MAX;
  u32x8 wf[2][8];
  {
    char* st = smem + w * (128 * 80);
    const u16* W = (w < 4 ? A.Wq : A.Wk) + h * 128 + (w & 3) * 32;
#pragma unroll
    for (int c = 0; c < 4; ++c) {
      u32x4 tmp[8];
#pragma unroll
      for (int i = 0; i < 8; ++i) {
        int row = (l >> 2) + 16 * i;
        int kc = 2 * c + (row >> 6);
        tmp[i] = *(const u32x4*)(W + (size_t)(kc * 64 + (row & 63)) * 4096 + (l & 3) * 8);
      }
#pragma unroll
      for (int i = 0; i < 8; ++i) *(u32x4*)(st + ((l >> 2) + 16 * i) * 80 + (l & 3) * 16) = tmp[i];
      asm volatile("s_waitcnt lgkmcnt(0)" ::: "memory");
      const int q = (l & 15) >> 2, p = l & 3;
      const int rbase = 16 * (g >> 1) + 4 * (g & 1) + (q & 1) + 8 * (q >> 1);
#pragma unroll
      for (int sl = 0; sl < 2; ++sl)
#pragma unroll
        for (int tt = 0; tt < 2; ++tt)
#pragma unroll
          for (int u = 0; u < 4; ++u) {
            int row = sl * 64 + rbase + 32 * (u >> 1) + 2 * (u & 1);
            s16x4 r = __builtin_amdgcn_ds_read_tr16_b64_v4i16((lds_s16x4_p)(st + row * 80 + (tt * 16 + p * 4) * 2));
            u32x2 r2 = __builtin_bit_cast(u32x2, r);
            wf[tt][2 * c + sl][2 * u] = r2.x; wf[tt][2 * c + sl][2 * u + 1] = r2.y;
          }
      asm volatile("s_waitcnt lgkmcnt(0)" ::: "memory");
    }
  }
  wait_flags(rs, FL_SEL, 1, 0, l, lim);
  u32 sel = __builtin_amdgcn_readfirstlane(ld4(rs, FL_SEL)) & 1;
  if (MODEV & 1) sel = 0;
  if (MODEV & 2) sel = 1;
  __syncthreads();
  char* memL = smem;
  char* qkL = smem + 8192;
  const int idx = (l & 8) ? 0xEEEE : 0x4444;
  const u32 commB = OFF_COMM + sel * 8192;
  const u32 memOff = commB + rep * 16384 + w * 1024 + l * 16;
  const u32 pOff = commB + (32 + (h >> 3)) * 16384;
  const int a = (l >> 3) & 1, r8 = l & 7;
  const u32 pTok = pOff + (h & 7) * 512 + (g * 8 + r8) * 16 + a * 65536;
  const u32 pMM = pOff + 4096 + (h & 7) * 128 + r8 * 16 + ((g & 1) * 2 + a) * 65536;
  const float csc = 0.08838834764831845f * 1.4426950408889634f;
  const bool isT1 = (w == 1 || w == 2);
  const int tt1 = w - 1;
  char* KQL = smem + 16384;
  const u32 kqOff = (w < 6 ? OFF_KX : OFF_QX) + (u32)h * 8192 + ((w & 1) * 16 + (l & 15)) * 256 + g * 16;
  int qkdone = -1;
  u32x4 kq[4];
  if (w >= 4) {
    wait_prog(rs, FL_QK + h * FS, 1, 0, l, qkdone, lim);
#pragma unroll
    for (int i = 0; i < 4; ++i) kq[i] = ld16(rs, kqOff + i * 64);
  }
  u32x4 ptP = {0, 0, 0, 0}; u32x2 pmP = {0, 0};
  const u32 ptOff = OFF_PT + (u32)(h * 2 + (tt1 & 1)) * 1024 + l * 16;
  const u32 pmOff = OFF_PM + (u32)((h >> 2) * 2 + (tt1 & 1)) * 1024 + (h & 3) * 256 + (l & 15) * 16 + (g & 1) * 8;
  if (w >= 4) {
#pragma unroll
    for (int i = 0; i < 4; ++i) *(u32x4*)(KQL + ((w - 4) * 4 + i) * 1024 + l * 16) = kq[i];
  }
  bar();
  u32x4 kf[2][4];
  if (w == 0) {
#pragma unroll
    for (int tt = 0; tt < 2; ++tt)
#pragma unroll
      for (int ks = 0; ks < 4; ++ks) kf[tt][ks] = *(const u32x4*)(KQL + (tt * 4 + ks) * 1024 + l * 16);
  }
  u32x4 m = *(const u32x4*)(A.mem0 + (l & 7) * 512 + w * 64 + (l >> 3) * 8);
  for (int s = 0; s < 64; ++s) {
    *(u32x4*)(memL + w * 1024 + l * 16) = m;
    char* kqb = KQL + (s & 1) * 16384;
    bar();
    if (w >= 4 && s < 63 && !(MODEV & 32)) {
      wait_prog(rs, FL_QK + h * FS, 1, s + 1, l, qkdone, lim);
#pragma unroll
      for (int i = 0; i < 4; ++i) kq[i] = ld16(rs, kqOff + (u32)(s + 1) * (32 * 8192) + i * 64);
    }
    float v[4];
    project(wf, memL, l, idx, v, [&]() {});
    {
      *(u32x2*)(qkL + (w * 32 + (g >> 1) * 16 + (l & 15)) * 16 + (g & 1) * 8) = u32x2{pk2(v[0], v[1]), pk2(v[2], v[3])};
    }
    bar();
    if (isT1 && s > 0 && !(MODEV & 8)) {
      st16(rs, ptOff + (u32)(s - 1) * 65536, ptP);
      if (g < 2) st8(rs, pmOff + (u32)(s - 1) * 16384, pmP);
    }
    if (w == 3 && s >= 2 && l == 0 && !(MODEV & 256)) st4(rs, FL_XP + h * FS, (u32)(s - 2));
    if (w == 0 && s < 63) {
      u32x4 qf[4], mf[4];
#pragma unroll
      for (int ks = 0; ks < 4; ++ks) {
        qf[ks] = frag_tr<false>(qkL, 16, ks * 32, (l & 1) * 8, l);
      }
#pragma unroll
      for (int ks = 0; ks < 4; ++ks) mf[ks] = frag_tr<false>(qkL + 2048, 16, ks * 32, (l & 1) * 8, l);
      __builtin_amdgcn_sched_barrier(0);
      float ci = g >= 2 ? -1e30f : 0.f;
      f32x4 d0 = {ci, ci, ci, ci}, d1 = {0, 0, 0, 0}, d2 = {0, 0, 0, 0};
#pragma unroll
      for (int ks = 0; ks < 4; ++ks) { d1 = mma(kf[0][ks], qf[ks], d1); d2 = mma(kf[1][ks], qf[ks], d2); }
#pragma unroll
      for (int ks = 0; ks < 4; ++ks) d0 = mma(mf[ks], qf[ks], d0);
      float e0 = a ? d2[0] : d1[0], e1 = a ? d2[1] : d1[1], e2 = a ? d2[2] : d1[2], e3 = a ? d2[3] : d1[3];
      float e4 = a ? d0[2] : d0[0], e5 = a ? d0[3] : d0[1];
      float mx = max6_red(e0, e1, e2, e3, e4, e5);
      float mc = -mx * csc;
      e0 = __builtin_amdgcn_exp2f(fmaf(e0, csc, mc)); e1 = __builtin_amdgcn_exp2f(fmaf(e1, csc, mc));
      e2 = __builtin_amdgcn_exp2f(fmaf(e2, csc, mc)); e3 = __builtin_amdgcn_exp2f(fmaf(e3, csc, mc));
      e4 = __builtin_amdgcn_exp2f(fmaf(e4, csc, mc)); e5 = __builtin_amdgcn_exp2f(fmaf(e5, csc, mc));
      float sum = ((e0 + e1) + (e2 + e3)) + (e4 + e5);
      sum = red_sum4(sum);
      float rl = __builtin_amdgcn_rcpf(sum);
      rl = (s & 1) ? -rl : rl;
      u32x2 pt = {pk2(e0 * rl, e1 * rl), pk2(e2 * rl, e3 * rl)};
      u32 pm = pk2(e4 * rl, e5 * rl);
      const u32x4 gt = {lo8(pt.x), lo8(pt.y), hi8(pt.x), hi8(pt.y)};
      st16(rs, pTok, gt); st16(rs, pTok + 2 * 65536, gt);
      const u32 ml = lo8(pm), mh = hi8(pm);
      auto sl = __builtin_amdgcn_permlane16_swap(ml, ml, false, false);
      auto sh = __builtin_amdgcn_permlane16_swap(mh, mh, false, false);
      if (l < 32) st16(rs, pMM, u32x4{sl[0], sh[0], sl[1], sh[1]});
    }
    if (isT1 && !(MODEV & 8)) {
      __builtin_amdgcn_s_sleep(3);
      u32x4 mf[4], kt[2][4], qt[4];
#pragma unroll
      for (int ks = 0; ks < 4; ++ks) {
        qt[ks] = *(const u32x4*)(kqb + (8 + tt1 * 4 + ks) * 1024 + l * 16);
        kt[0][ks] = *(const u32x4*)(kqb + ks * 1024 + l * 16); kt[1][ks] = *(const u32x4*)(kqb + (4 + ks) * 1024 + l * 16);
        mf[ks] = frag_tr<false>(qkL + 2048, 16, ks * 32, (l & 1) * 8, l);
      }
      f32x4 d0 = {0, 0, 0, 0}, d1 = {0, 0, 0, 0}, d2 = {0, 0, 0, 0};
#pragma unroll
      for (int ks = 0; ks < 4; ++ks) { d1 = mma(kt[0][ks], qt[ks], d1); d2 = mma(kt[1][ks], qt[ks], d2); }
#pragma unroll
      for (int ks = 0; ks < 4; ++ks) d0 = mma(mf[ks], qt[ks], d0);
      const int qi = tt1 * 16 + (l & 15);
      float e[12];
#pragma unroll
      for (int r = 0; r < 4; ++r) {
        e[r] = (4 * g + r <= qi) ? d1[r] : -1e30f;
        e[4 + r] = (16 + 4 * g + r <= qi) ? d2[r] : -1e30f;
        e[8 + r] = (g < 2) ? d0[r] : -1e30f;
      }
      float mx = e[0];
#pragma unroll
      for (int r = 1; r < 12; ++r) mx = fmaxf(mx, e[r]);
      mx = max1632(mx);
      float mc = -mx * csc, sum = 0.f;
#pragma unroll
      for (int r = 0; r < 12; ++r) { e[r] = __builtin_amdgcn_exp2f(fmaf(e[r], csc, mc)); sum += e[r]; }
      sum = sum1632(sum);
      float rl = __builtin_amdgcn_rcpf(sum);
      ptP = u32x4{pk2(e[0] * rl, e[1] * rl), pk2(e[2] * rl, e[3] * rl), pk2(e[4] * rl, e[5] * rl), pk2(e[6] * rl, e[7] * rl)};
      pmP = u32x2{pk2(e[8] * rl, e[9] * rl), pk2(e[10] * rl, e[11] * rl)};
    }
    if (s < 63) {
      char* kqn = KQL + ((s + 1) & 1) * 16384;
      if (w >= 4) {
        __builtin_amdgcn_s_sleep(4);
        wait_prog(rs, FL_QK + h * FS, 1, min(s + 2, 63), l, qkdone, lim);
#pragma unroll
        for (int i = 0; i < 4; ++i) *(u32x4*)(kqn + ((w - 4) * 4 + i) * 1024 + l * 16) = kq[i];
      }
      bar();
      if (w == 0) {
#pragma unroll
        for (int tt = 0; tt < 2; ++tt)
#pragma unroll
          for (int ks = 0; ks < 4; ++ks) kf[tt][ks] = *(const u32x4*)(kqn + (tt * 4 + ks) * 1024 + l * 16);
      }
      m = poll_mem(rs, memOff + ((s + 1) & 3) * 131072, lim); drain();
    }
  }
  if (isT1) {
    st16(rs, ptOff + 63u * 65536, ptP);
    if (g < 2) st8(rs, pmOff + 63u * 16384, pmP);
  }
  drain();
  bar();
  if (t == 192) st4(rs, FL_XP + h * FS, 63u);
}

DI void fold_task(const Args& A, char* smem, rsrc_t rs, int task) {
  const int t = threadIdx.x, l = t & 63, w = __builtin_amdgcn_readfirstlane(t >> 6);
  const int g = l >> 4, h = task >> 2, q = task & 3;
  const int r = l & 15, gr = r >> 2, rg = r & 3;
  const int drow = 64 * w + 16 * (gr >> 1) + 4 * (gr & 1) + 8 * (rg >> 1) + (rg & 1);
  const u16* Wv = A.Wv + (size_t)drow * 4096 + h * 128 + g * 8;
  u32x4 tmp[4], af[4][4];
  slab_load<16, 4>(A.Wo + (size_t)h * 128 * 512 + 128 * q, 512, t, tmp);
#pragma unroll
  for (int u = 0; u < 4; ++u)
#pragma unroll
    for (int ks = 0; ks < 4; ++ks) af[u][ks] = *(const u32x4*)(Wv + (size_t)(32 * (u >> 1) + 2 * (u & 1)) * 4096 + ks * 32);
  __builtin_amdgcn_sched_barrier(0);
  slab_store<16, 4>(smem, t, tmp);
  __syncthreads();
#pragma unroll
  for (int ct = 0; ct < 8; ++ct) {
    u32x4 wo[4];
#pragma unroll
    for (int ks = 0; ks < 4; ++ks) wo[ks] = frag_tr<false>(smem, 272, ks * 32, (16 * ct + 4 * (l & 3)) * 2, l);
    const int c = 128 * q + 16 * ct + r, cc = c >> 3, jj = c & 7;
#pragma unroll
    for (int half = 0; half < 2; ++half) {
      f32x4 a0 = {0, 0, 0, 0}, a1 = {0, 0, 0, 0};
#pragma unroll
      for (int ks = 0; ks < 4; ++ks) { a0 = mma(af[2 * half][ks], wo[ks], a0); a1 = mma(af[2 * half + 1][ks], wo[ks], a1); }
      const u32 o = (u32)((((((cc * 8 + (h >> 2)) * 2 + ((h >> 1) & 1)) * 8 + w) * 2 + half) * 64 + g * 16 + (h & 1) * 8 + jj)) * 16;
      st16(rs, OFF_BF + o, u32x4{pk2(a0[0], a0[1]), pk2(a0[2], a0[3]), pk2(a1[0], a1[1]), pk2(a1[2], a1[3])});
    }
  }
  drain();
  __syncthreads();
  if (t == 0) st4(rs, FL_FOLD + task * FS, 1u);
}

DI void ln_task(const Args& A, rsrc_t rs, int s0) {
  const int t = threadIdx.x, l = t & 63, w = __builtin_amdgcn_readfirstlane(t >> 6);
  const int g = l & 3, tk = (l >> 2) & 3, kq = l >> 4, tok = 4 * w + tk;
  u32x4 v[2][4];
#pragma unroll
  for (int u = 0; u < 2; ++u)
#pragma unroll
    for (int i = 0; i < 4; ++i) v[u][i] = *(const u32x4*)(A.x + (size_t)(32 * (s0 + 32 * u) + tok) * 512 + 32 * (4 * i + kq) + 8 * g);
#pragma unroll
  for (int u = 0; u < 2; ++u) {
    f32x2 c[16]; float var;
    ln_center(v[u], c, var);
    float r = __builtin_amdgcn_rsqf(var + 1e-5f);
#pragma unroll
    for (int i = 0; i < 4; ++i)
      st16(rs, OFF_XN + (u32)(s0 + 32 * u) * 32768 + (u32)((((tok >> 4) * 16 + 4 * i + kq) * 64) + g * 16 + ((tok & 15) ^ (4 * g))) * 16, ln_pack(c, i, r));
  }
  drain();
  __syncthreads();
  if (t < 2) st4(rs, FL_XN + (s0 + 32 * t) * FS, 1u);
}

DI void role_Y(const Args& A, char* smem, rsrc_t rs, int cc, int rep) {
  const int t = threadIdx.x, l = t & 63, w = __builtin_amdgcn_readfirstlane(t >> 6);
  const int g = l >> 4;
  int lim = SPIN_MAX;
  fold_task(A, smem, rs, cc);
  wait_flags(rs, FL_FOLD, 64, 0, l, lim);
  wait_flags(rs, FL_FOLD + 64 * FS, 64, 0, l, lim);
  u32x8 wf[2][8];
#pragma unroll
  for (int tt = 0; tt < 2; ++tt)
#pragma unroll
    for (int i = 0; i < 8; ++i) {
      int kc = i;
      u32 o = OFF_BF + (u32)((((cc * 8 + w) * 2 + tt) * 8 + kc) * 2) * 1024 + l * 16;
      u32x4 lo = ld16(rs, o), hi = ld16(rs, o + 1024);
      wf[tt][i] = u32x8{lo.x, lo.y, lo.z, lo.w, hi.x, hi.y, hi.z, hi.w};
    }
  wait_flags(rs, FL_SEL, 1, 0, l, lim);
  u32 sel = __builtin_amdgcn_readfirstlane(ld4(rs, FL_SEL)) & 1;
  if (MODEV & 1) sel = 0;
  if (MODEV & 2) sel = 1;
  char* memL = smem;
  char* YL = smem + 8192;
  float* RL = (float*)(smem + 12288);
  const int idx = (l & 8) ? 0xEEEE : 0x4444;
  const u32 commB = OFF_COMM + sel * 8192;
  const u32 memOff = commB + (rep + YREP) * 16384 + w * 1024 + l * 16;
  const int set = (l >> 3) & 1, r8 = l & 7;
  const u32 pBase = commB + (32 + rep * 4 + (w & 3)) * 16384;
  const u32 pTok = pBase + set * 512 + (g * 8 + r8) * 16;
  const u32 pMM = pBase + 4096 + (set * 4 + g) * 128 + r8 * 16;
  const u32 uOff = OFF_U + (u32)cc * 16384 + (u32)(8 * (w & 3) + set) * 512 + g * 128 + r8 * 16;
  char* UL = smem + 16384;
  int vudone = -1;
  u32x4 un[4];
  if (w >= 4) {
    wait_prog(rs, FL_VU, 32, 0, l, vudone, lim);
#pragma unroll
    for (int mm = 0; mm < 4; ++mm) un[mm] = ld16(rs, uOff + mm * 1024);
  }
  u32x4 m = *(const u32x4*)(A.mem0 + (l & 7) * 512 + w * 64 + (l >> 3) * 8);
  const int rlIdx = (l & 7) * 8 + ((l >> 4) & 1) + 2 * (l >> 5) + 4 * ((l >> 3) & 1);
  const u32 pubOff = commB + (l >> 3) * 16384 + cc * 128 + (l & 7) * 16;
  u32 tref = now32();
  int dP = DYV;
  for (int s = 0; s < 64; ++s) {
    *(u32x4*)(memL + w * 1024 + l * 16) = m;
    char* ub = UL + (s & 1) * 16384 + (w & 3) * 4096 + l * 16;
    if (w >= 4) {
#pragma unroll
      for (int mm = 0; mm < 4; ++mm) *(u32x4*)(ub + mm * 1024) = un[mm];
    }
    bar();
    if (w >= 4 && s < 62 && !(MODEV & 16)) {
      wait_prog(rs, FL_VU, 32, s + 1, l, vudone, lim);
#pragma unroll
      for (int mm = 0; mm < 4; ++mm) un[mm] = ld16(rs, uOff + (u32)(s + 1) * (64 * 16384) + mm * 1024);
    }
    if (w == 5 && s >= 2)
      st16(rs, commB + (((s - 1) & 3) * 8 + (l >> 3)) * 16384 + cc * 128 + (l & 7) * 16, u32x4{0xFFFFFFFFu, 0xFFFFFFFFu, 0xFFFFFFFFu, 0xFFFFFFFFu});
    float v[4];
    u32x4 uf[4];
    project(wf, memL, l, idx, v, [&]() {
      if (w < 4) {
#pragma unroll
        for (int mm = 0; mm < 4; ++mm) uf[mm] = *(const u32x4*)(ub + mm * 1024);
      }
    });
    {
      u32x2 pk = {pk2(v[0], v[1]), pk2(v[2], v[3])};
      int head = 4 * w + 2 * (g >> 1) + ((l >> 3) & 1);
      *(u32x2*)(YL + (head * 8 + (l & 7)) * 16 + (g & 1) * 8) = pk;
    }
    bar();
    if (w >= 5 && s < 61) wait_prog(rs, FL_VU, 32, s + 2, l, vudone, lim);
    if (w == 4 && !(MODEV & 128)) {
#pragma unroll
      for (int i = 0; i < 4; ++i) {
        int id = i * 64 + l;
        u32x4 y = *(const u32x4*)(YL + id * 16);
        st16(rs, OFF_YP + (u32)((s * 32 + (id >> 3)) * 512 + cc * 8 + (id & 7)) * 16, y);
      }
    }
    if (w == 4 && s < 61) wait_prog(rs, FL_VU, 32, s + 2, l, vudone, lim);
    if (s < 63) {
      if (w < 4) {
        u32x4 yf = *(const u32x4*)(YL + ((8 * w + 4 * set + g) * 8 + r8) * 16);
        u32x4 pf[5];
        wait_for(tref, dP);
        const u32 tm = (s & 1) ? 0x80008000u : 0u;
        bool ok0 = false, ok1 = false, ok2 = false, ok3 = false, ok4 = false;
        int it = 0;
        for (; it < lim; ++it) {
          if (!ok0) pf[0] = ld16(rs, pTok);
          if (!ok1) pf[1] = ld16(rs, pTok + 1024);
          if (!ok2) pf[2] = ld16(rs, pTok + 2048);
          if (!ok3) pf[3] = ld16(rs, pTok + 3072);
          if (!ok4) pf[4] = ld16(rs, pMM);
          ok0 = (((pf[0].x ^ tm) | (pf[0].z ^ tm)) & 0x80008000u) == 0;
          ok1 = (((pf[1].x ^ tm) | (pf[1].z ^ tm)) & 0x80008000u) == 0;
          ok2 = (((pf[2].x ^ tm) | (pf[2].z ^ tm)) & 0x80008000u) == 0;
          ok3 = (((pf[3].x ^ tm) | (pf[3].z ^ tm)) & 0x80008000u) == 0;
          ok4 = (((pf[4].x ^ tm) | (pf[4].y ^ tm) | (pf[4].z ^ tm) | (pf[4].w ^ tm)) & 0x80008000u) == 0;
          if (__all(ok0 && ok1 && ok2 && ok3 && ok4)) break;
          asm volatile("" ::: "memory");
        }
        if (it >= lim) lim = 1;
        dP += (it == 0) ? -ADEC : min(it, 4) * AINC;
        dP = max(min(dP, 6000), 0);
        f32x4 d = {0, 0, 0, 0}, d2 = {0, 0, 0, 0};
        d = mma(pf[0], uf[0], d); d2 = mma(pf[1], uf[1], d2); d = mma(pf[2], uf[2], d); d2 = mma(pf[3], uf[3], d2); d = mma(pf[4], yf, d);
        d += d2;
        if ((g >> 1) == set) {
          float* dst = RL + (w * 2 + set) * 64 + (4 * (g & 1)) * 8 + r8;
          dst[0] = d[0]; dst[8] = d[1]; dst[16] = d[2]; dst[24] = d[3];
        }
      }
      bar();
      if (w == 0) {
        const float* R = RL + rlIdx;
        float x0 = R[0], x1 = R[64], x2 = R[128], x3 = R[192], x4 = R[256], x5 = R[320], x6 = R[384], x7 = R[448];
        float sum = ((x0 + x1) + (x2 + x3)) + ((x4 + x5) + (x6 + x7));
        auto s16 = __builtin_amdgcn_permlane16_swap(__float_as_uint(sum), __float_as_uint(sum), false, false);
        u32 pk = pk2(__uint_as_float(s16[0]), __uint_as_float(s16[1]));
        if (s & 1) pk ^= 0x80008000u;
        pk = min(pk, 0xFFFFFFFEu);
        auto s32 = __builtin_amdgcn_permlane32_swap(pk, pk, false, false);
        u32x4 o = {lo8(s32[0]), lo8(s32[1]), hi8(s32[0]), hi8(s32[1])};
        if (l < 32 || YREP) st16(rs, pubOff + ((s + 1) & 3) * 131072, o);
      }
      const u32 tpub = now32();
      wait_for(tpub, DYMV);
      m = poll_mem(rs, memOff + ((s + 1) & 3) * 131072, lim);
      tref = now32();
      drain();
    }
  }
  drain();
  bar();
  if (t == 0) st4(rs, FL_YE + cc * FS, 1u);
}

DI void role_QK(const Args& A, char* smem, rsrc_t rs, int h) {
  const int t = threadIdx.x, l = t & 63, w = __builtin_amdgcn_readfirstlane(t >> 6);
  const int g = l >> 4;
  int lim = SPIN_MAX;
  u32x4 wf[2][16];
#pragma unroll
  for (int rnd = 0; rnd < 2; ++rnd) {
    u32x4 tmp[16];
#pragma unroll
    for (int i = 0; i < 16; ++i) {
      int id = i * 512 + t, row = id >> 5, c8 = id & 31;
      tmp[i] = *(const u32x4*)((c8 < 16 ? A.Wq : A.Wk) + (size_t)(rnd * 256 + row) * 4096 + h * 128 + (c8 & 15) * 8);
    }
    slab_store<32, 16>(smem, t, tmp);
    __syncthreads();
#pragma unroll
    for (int ks = 0; ks < 8; ++ks)
#pragma unroll
      for (int te = 0; te < 2; ++te) wf[te][rnd * 8 + ks] = frag_tr<false>(smem, 528, ks * 32, (32 * w + 8 * (l & 3) + 4 * te) * 2, l);
    __syncthreads();
  }
  if (w == 0) wait_flags(rs, FL_XN, 8, 0, l, lim);
  __syncthreads();
  u32x4 pf[4];
#pragma unroll
  for (int i = 0; i < 4; ++i) pf[i] = ld16(rs, OFF_XN + (i * 512 + t) * 16);
#pragma unroll
  for (int i = 0; i < 4; ++i) *(u32x4*)(smem + (i * 512 + t) * 16) = pf[i];
  bar();
  const int slot = (g * 16 + ((l & 15) ^ (4 * g))) * 16;
  const u32 oOff = (w < 4 ? OFF_QX : OFF_KX) + (u32)h * 8192 + (l & 15) * 256 + ((w & 3) * 32 + 8 * g) * 2;
  for (int s = 0; s < 64; ++s) {
    if (s == 6) { if (w == 0) wait_flags(rs, FL_XN, 64, 0, l, lim); bar(); }
#pragma unroll
    for (int i = 0; i < 4; ++i) pf[i] = ld16(rs, OFF_XN + (u32)min(s + 1, 63) * 32768 + (i * 512 + t) * 16);
    __builtin_amdgcn_sched_barrier(0);
    const char* xb = smem + (s & 1) * 32768 + slot;
    f32x4 acc[2][2] = {};
    u32x4 bb[2][4][2];
#pragma unroll
    for (int k = 0; k < 4; ++k) { bb[0][k][0] = *(const u32x4*)(xb + k * 1024); bb[0][k][1] = *(const u32x4*)(xb + 16384 + k * 1024); }
#pragma unroll
    for (int gq = 0; gq < 4; ++gq) {
      if (gq < 3) {
#pragma unroll
        for (int k = 0; k < 4; ++k) {
          bb[(gq + 1) & 1][k][0] = *(const u32x4*)(xb + ((gq + 1) * 4 + k) * 1024);
          bb[(gq + 1) & 1][k][1] = *(const u32x4*)(xb + 16384 + ((gq + 1) * 4 + k) * 1024);
        }
      }
      __builtin_amdgcn_sched_barrier(0);
#pragma unroll
      for (int k = 0; k < 4; ++k) {
        const int ks = gq * 4 + k;
        acc[0][0] = mma(wf[0][ks], bb[gq & 1][k][0], acc[0][0]); acc[1][0] = mma(wf[1][ks], bb[gq & 1][k][0], acc[1][0]);
        acc[0][1] = mma(wf[0][ks], bb[gq & 1][k][1], acc[0][1]); acc[1][1] = mma(wf[1][ks], bb[gq & 1][k][1], acc[1][1]);
      }
      __builtin_amdgcn_sched_barrier(0);
    }
    drain();
#pragma unroll
    for (int i = 0; i < 4; ++i) *(u32x4*)(smem + ((s + 1) & 1) * 32768 + (i * 512 + t) * 16) = pf[i];
#pragma unroll
    for (int tk = 0; tk < 2; ++tk)
      st16(rs, oOff + (u32)s * (32 * 8192) + tk * 4096,
           u32x4{pk2(acc[0][tk][0], acc[0][tk][1]), pk2(acc[0][tk][2], acc[0][tk][3]), pk2(acc[1][tk][0], acc[1][tk][1]), pk2(acc[1][tk][2], acc[1][tk][3])});
    if (s <= 2 || s == 63) drain();
    bar();
    if (t == 0) st4(rs, FL_QK + h * FS, (u32)((s <= 2 || s == 63) ? s : s - 1));
  }
}

DI void role_VU(const Args& A, char* smem, rsrc_t rs, int h) {
  const int t = threadIdx.x, l = t & 63, w = __builtin_amdgcn_readfirstlane(t >> 6);
  const int g = l >> 4;
  int lim = SPIN_MAX;
  u32x4 wv[16], wo[4][4];
  {
    u32x4 tmp[16];
    slab_load<16, 16>(A.Wv + h * 128, 4096, t, tmp);
    slab_store<16, 16>(smem, t, tmp);
    __syncthreads();
    slab_load<64, 16>(A.Wo + (size_t)h * 128 * 512, 512, t, tmp);
#pragma unroll
    for (int ks = 0; ks < 16; ++ks) wv[ks] = frag_tr<false>(smem, 272, ks * 32, (16 * w + 4 * (l & 3)) * 2, l);
    __syncthreads();
    slab_store<64, 16>(smem, t, tmp);
    __syncthreads();
#pragma unroll
    for (int ct = 0; ct < 4; ++ct)
#pragma unroll
      for (int ks = 0; ks < 4; ++ks) wo[ct][ks] = frag_tr<true>(smem, 1040, ks * 32, (64 * w + 16 * ct + 4 * (l & 3)) * 2, l);
    __syncthreads();
  }
  if (w == 0) wait_flags(rs, FL_XN, 8, 0, l, lim);
  __syncthreads();
  u32x4 pf[4];
#pragma unroll
  for (int i = 0; i < 4; ++i) pf[i] = ld16(rs, OFF_XN + (i * 512 + t) * 16);
#pragma unroll
  for (int i = 0; i < 4; ++i) *(u32x4*)(smem + (i * 512 + t) * 16) = pf[i];
  bar();
  char* VL = smem + 32768;
  const int slot = (g * 16 + ((l & 15) ^ (4 * g))) * 16;
  const u32 uOff = OFF_U + (u32)(8 * w + ((l >> 3) & 1)) * 16384 + (u32)h * 512 + g * 128 + (l & 7) * 16;
  for (int s = 0; s < 64; ++s) {
    if (s == 6) { if (w == 0) wait_flags(rs, FL_XN, 64, 0, l, lim); bar(); }
#pragma unroll
    for (int i = 0; i < 4; ++i) pf[i] = ld16(rs, OFF_XN + (u32)min(s + 1, 63) * 32768 + (i * 512 + t) * 16);
    __builtin_amdgcn_sched_barrier(0);
    const char* xb = smem + slot;
    f32x4 av[2] = {};
    u32x4 bb[2][4][2];
#pragma unroll
    for (int k = 0; k < 4; ++k) { bb[0][k][0] = *(const u32x4*)(xb + k * 1024); bb[0][k][1] = *(const u32x4*)(xb + 16384 + k * 1024); }
#pragma unroll
    for (int gq = 0; gq < 4; ++gq) {
      if (gq < 3) {
#pragma unroll
        for (int k = 0; k < 4; ++k) {
          bb[(gq + 1) & 1][k][0] = *(const u32x4*)(xb + ((gq + 1) * 4 + k) * 1024);
          bb[(gq + 1) & 1][k][1] = *(const u32x4*)(xb + 16384 + ((gq + 1) * 4 + k) * 1024);
        }
      }
      __builtin_amdgcn_sched_barrier(0);
#pragma unroll
      for (int k = 0; k < 4; ++k) { av[0] = mma(wv[gq * 4 + k], bb[gq & 1][k][0], av[0]); av[1] = mma(wv[gq * 4 + k], bb[gq & 1][k][1], av[1]); }
      __builtin_amdgcn_sched_barrier(0);
    }
#pragma unroll
    for (int tk = 0; tk < 2; ++tk)
      *(u32x2*)(VL + (tk * 4 + (w >> 1)) * 1024 + l * 16 + (w & 1) * 8) = u32x2{pk2(av[tk][0], av[tk][1]), pk2(av[tk][2], av[tk][3])};
    bar();
    u32x4 vf[2][4];
#pragma unroll
    for (int tk = 0; tk < 2; ++tk)
#pragma unroll
      for (int ks = 0; ks < 4; ++ks) vf[tk][ks] = *(const u32x4*)(VL + (tk * 4 + ks) * 1024 + l * 16);
    drain();
#pragma unroll
    for (int i = 0; i < 4; ++i) *(u32x4*)(smem + (i * 512 + t) * 16) = pf[i];
#pragma unroll
    for (int ct = 0; ct < 4; ++ct) {
      f32x4 a0 = {0, 0, 0, 0}, a1 = {0, 0, 0, 0};
#pragma unroll
      for (int ks = 0; ks < 4; ++ks) { a0 = mma(vf[0][ks], wo[ct][ks], a0); a1 = mma(vf[1][ks], wo[ct][ks], a1); }
      st16(rs, uOff + (u32)s * (64 * 16384) + ct * 2 * 16384, u32x4{pk2(a0[0], a0[1]), pk2(a0[2], a0[3]), pk2(a1[0], a1[1]), pk2(a1[2], a1[3])});
    }
    if (s <= 2 || s == 63) drain();
    bar();
    if (t == 0) st4(rs, FL_VU + h * FS, (u32)((s <= 2 || s == 63) ? s : s - 1));
  }
}

DI f32x4 reduce8(char* smem, const f32x4 (&acc)[4][2], int w, int l) {
#pragma unroll
  for (int ct = 0; ct < 4; ++ct)
#pragma unroll
    for (int tk = 0; tk < 2; ++tk) *(f32x4*)(smem + ((w * 8 + ct * 2 + tk) * 64 + l) * 16) = acc[ct][tk];
  bar();
  f32x4 p[8];
#pragma unroll
  for (int i = 0; i < 8; ++i) p[i] = *(const f32x4*)(smem + ((i * 8 + w) * 64 + l) * 16);
  return ((p[0] + p[1]) + (p[2] + p[3])) + ((p[4] + p[5]) + (p[6] + p[7]));
}

DI void role_T2(const Args& A, char* smem, rsrc_t rs, int gi, int j) {
  const int t = threadIdx.x, l = t & 63, w = __builtin_amdgcn_readfirstlane(t >> 6);
  const int g = l >> 4, r = l & 15;
  int lim = SPIN_MAX, vudone = -1;
  const u32 uOff = OFF_U + (u32)(8 * j + (r >> 2)) * 16384 + (u32)(4 * w) * 512 + g * 128 + (r & 3) * 16;
  const u32 yOff = OFF_YP + (u32)((4 * w + g) * 512 + 64 * j + 8 * (r >> 2) + (r & 3)) * 16;
  const int ct = w >> 1, tk = w & 1;
  const int ecol = 64 * j + 32 * (ct >> 1) + 8 * g + 4 * (ct & 1);
  for (int s = gi; s < 64; s += 4) {
    if (vudone < s) { if (w == 0) wait_prog(rs, FL_VU, 32, s, l, vudone, lim); bar(); vudone = s; if (w == 0) vudone = s; }
    u32x4 uf[4][4];
#pragma unroll
    for (int hh = 0; hh < 4; ++hh)
#pragma unroll
      for (int c = 0; c < 4; ++c) uf[hh][c] = ld16(rs, uOff + (u32)s * (64 * 16384) + (c >> 1) * 4 * 16384 + (c & 1) * 64 + hh * 512);
    const int tok = 32 * s + 16 * tk + r;
    u32x2 xv = *(const u32x2*)(A.x + (size_t)tok * 512 + ecol);
    if (w == 0) { wait_flags_bk(rs, FL_XP, 32, s, l, lim); if (s >= 62) wait_flags(rs, FL_YE + 8 * j * FS, 8, 0, l, lim); }
    bar();
    u32x4 pf[4][2], pm[2], yf[4];
#pragma unroll
    for (int hh = 0; hh < 4; ++hh)
#pragma unroll
      for (int k = 0; k < 2; ++k) pf[hh][k] = ld16(rs, OFF_PT + (u32)((s * 32 + 4 * w + hh) * 2 + k) * 1024 + l * 16);
#pragma unroll
    for (int k = 0; k < 2; ++k) pm[k] = ld16(rs, OFF_PM + (u32)((s * 8 + w) * 2 + k) * 1024 + l * 16);
#pragma unroll
    for (int c = 0; c < 4; ++c) yf[c] = ld16(rs, yOff + (u32)s * (32 * 512 * 16) + ((c >> 1) * 32 + (c & 1) * 4) * 16);
    f32x4 acc[4][2] = {};
#pragma unroll
    for (int hh = 0; hh < 4; ++hh)
#pragma unroll
      for (int c = 0; c < 4; ++c) { acc[c][0] = mma(uf[hh][c], pf[hh][0], acc[c][0]); acc[c][1] = mma(uf[hh][c], pf[hh][1], acc[c][1]); }
#pragma unroll
    for (int c = 0; c < 4; ++c) { acc[c][0] = mma(yf[c], pm[0], acc[c][0]); acc[c][1] = mma(yf[c], pm[1], acc[c][1]); }
    f32x4 o = reduce8(smem, acc, w, l);
    u32 a0 = pk2(o[0], o[1]), a1 = pk2(o[2], o[3]);
    u32x2 hv = {pk2(lo16(xv.x) + lo16(a0), hi16(xv.x) + hi16(a0)), pk2(lo16(xv.y) + lo16(a1), hi16(xv.y) + hi16(a1))};
    st8(rs, OFF_H + (u32)(ecol >> 5) * KBS + (u32)tok * 64 + (ecol & 31) * 2, hv);
    drain();
    bar();
    if (t == 0) st4(rs, FL_T2 + (gi * 8 + j) * FS, (u32)s);
  }
}

template <int MODE>
DI void role_B128(const Args& A, char* smem, rsrc_t rs, int gi, int j) {
  const int t = threadIdx.x, l = t & 63, w = __builtin_amdgcn_readfirstlane(t >> 6);
  const int g = l >> 4, r = l & 15, cg = w & 3, tk = w >> 2;
  int lim = SPIN_MAX;
  u32x4 wf[2][16];
  {
    u32x4 tmp[16];
    const u16* W = (MODE == 0 ? A.w1 : MODE == 1 ? A.w2 : A.w3) + 128 * j;
    slab_load<16, 16>(W, MODE == 2 ? 1792 : 512, t, tmp);
    slab_store<16, 16>(smem, t, tmp);
    __syncthreads();
#pragma unroll
    for (int ks = 0; ks < 16; ++ks)
#pragma unroll
      for (int te = 0; te < 2; ++te) wf[te][ks] = frag_tr<false>(smem, 272, ks * 32, (32 * cg + 8 * (l & 3) + 4 * te) * 2, l);
    __syncthreads();
  }
  const int lg = l & 3, ltk = (l >> 2) & 3, lkq = l >> 4, ltok = 4 * w + ltk;
  const int rslot = (MODE == 1) ? l * 16 : (g * 16 + (r ^ (4 * g))) * 16;
  for (int s = gi; s < 64; s += 2) {
    u32x4 yv;
    if (MODE == 1) {
      if (w == 0) {
        wait_flags(rs, FL_B1 + (s & 1) * 4 * FS, 4, s, l, lim, SLPV);
        wait_flags(rs, FL_B1 + ((s + 1) & 1) * 4 * FS, 4, s < 63 ? s + 1 : s - 1, l, lim, SLPV);
      }
      bar();
      u32x4 v[4];
#pragma unroll
      for (int i = 0; i < 4; ++i) {
        const int f = 4 * w + i, ks = f & 15, grp = ks >> 2;
        const int sh = (grp == 0) ? -1 : (grp == 1) ? 1 : (grp == 2) ? -2 : 2;
        const int row = 32 * s + 16 * (f >> 4) + r + sh;
        const bool okr = row >= 0 && row < 2048;
        u32x4 z = ld16(rs, OFF_H1 + (u32)ks * KBS + (u32)(okr ? row : 0) * 64 + g * 16);
        v[i] = okr ? z : u32x4{0, 0, 0, 0};
      }
      yv = ld16(rs, OFF_YB + (u32)(4 * j + cg) * KBS + (u32)(32 * s + 16 * tk + r) * 64 + g * 16);
#pragma unroll
      for (int i = 0; i < 4; ++i) *(u32x4*)(smem + (4 * w + i) * 1024 + l * 16) = v[i];
    } else {
      if (w == 0) {
        if (MODE == 0) wait_flags(rs, FL_T2 + (s & 3) * 8 * FS, 8, s, l, lim, SLPV);
        else wait_flags(rs, FL_B2A + gi * 4 * FS, 4, s, l, lim, SLPV);
      }
      bar();
      u32x4 v[4];
#pragma unroll
      for (int i = 0; i < 4; ++i) v[i] = ld16(rs, (MODE == 0 ? OFF_H : OFF_X1) + (u32)(4 * i + lkq) * KBS + (u32)(32 * s + ltok) * 64 + lg * 16);
      f32x2 c[16]; float var;
      ln_center(v, c, var);
      float r1 = __builtin_amdgcn_rsqf(var + 1e-5f), rz = r1;
      if (MODE == 0) {
        rz = r1 * __builtin_amdgcn_rsqf(var * r1 * r1 + 1e-5f);
#pragma unroll
        for (int i = 0; i < 4; ++i)
          if (i == j) st16(rs, OFF_YB + (u32)(4 * i + lkq) * KBS + (u32)(32 * s + ltok) * 64 + lg * 16, ln_pack(c, i, r1));
      }
#pragma unroll
      for (int i = 0; i < 4; ++i)
        *(u32x4*)(smem + (((ltok >> 4) * 16 + 4 * i + lkq) * 64 + lg * 16 + ((ltok & 15) ^ (4 * lg))) * 16) = ln_pack(c, i, rz);
    }
    bar();
    f32x4 a0 = {0, 0, 0, 0}, a1 = {0, 0, 0, 0};
    const char* xb = smem + tk * 16384 + rslot;
#pragma unroll
    for (int ks = 0; ks < 16; ++ks) {
      u32x4 b = *(const u32x4*)(xb + ks * 1024);
      a0 = mma(wf[0][ks], b, a0); a1 = mma(wf[1][ks], b, a1);
    }
    u32x4 o;
    if (MODE == 1) {
      u32 p0 = pk2(a0[0], a0[1]), p1 = pk2(a0[2], a0[3]), p2 = pk2(a1[0], a1[1]), p3 = pk2(a1[2], a1[3]);
      o = u32x4{pk2(lo16(yv.x) + lo16(p0), hi16(yv.x) + hi16(p0)), pk2(lo16(yv.y) + lo16(p1), hi16(yv.y) + hi16(p1)),
                pk2(lo16(yv.z) + lo16(p2), hi16(yv.z) + hi16(p2)), pk2(lo16(yv.w) + lo16(p3), hi16(yv.w) + hi16(p3))};
    } else {
      o = u32x4{pk2(gelu_t(a0[0]), gelu_t(a0[1])), pk2(gelu_t(a0[2]), gelu_t(a0[3])), pk2(gelu_t(a1[0]), gelu_t(a1[1])), pk2(gelu_t(a1[2]), gelu_t(a1[3]))};
    }
    st16(rs, (MODE == 0 ? OFF_H1 : MODE == 1 ? OFF_X1 : OFF_F) + (u32)(4 * j + cg) * KBS + (u32)(32 * s + 16 * tk + r) * 64 + g * 16, o);
    drain();
    bar();
    if (t == 0) st4(rs, (MODE == 0 ? FL_B1 + gi * 4 * FS : MODE == 1 ? FL_B2A + gi * 4 * FS : FL_B2B + gi * 16 * FS) + j * FS, (u32)s);
  }
}

DI void role_B2c(const Args& A, char* smem, rsrc_t rs, int gi, int j) {
  const int t = threadIdx.x, l = t & 63, w = __builtin_amdgcn_readfirstlane(t >> 6);
  const int g = l >> 4, r = l & 15;
  int lim = SPIN_MAX;
  u32x4 wf[4][7];
#pragma unroll
  for (int rnd = 0; rnd < 2; ++rnd) {
    u32x4 tmp[14];
    slab_load<8, 14>(A.w4 + (size_t)rnd * 896 * 512 + 64 * j, 512, t, tmp);
    slab_store<8, 14>(smem, t, tmp);
    __syncthreads();
    if ((w >> 2) == rnd) {
#pragma unroll
      for (int ks = 0; ks < 7; ++ks)
#pragma unroll
        for (int c = 0; c < 4; ++c) wf[c][ks] = frag_tr<false>(smem, 144, (w & 3) * 224 + ks * 32, (32 * (c >> 1) + 8 * (l & 3) + 4 * (c & 1)) * 2, l);
    }
    __syncthreads();
  }
  const int ct = w >> 1, tk = w & 1;
  const int ecol = 64 * j + 32 * (ct >> 1) + 8 * g + 4 * (ct & 1);
  for (int s = gi; s < 64; s += 2) {
    if (w == 0) wait_flags(rs, FL_B2B + gi * 16 * FS, 14, s, l, lim, SLPV);
    bar();
    u32x4 ff[2][7];
#pragma unroll
    for (int k = 0; k < 2; ++k)
#pragma unroll
      for (int ks = 0; ks < 7; ++ks) ff[k][ks] = ld16(rs, OFF_F + (u32)(7 * w + ks) * KBS + (u32)(32 * s + 16 * k + r) * 64 + g * 16);
    const int tok = 32 * s + 16 * tk + r;
    const u32 eo = (u32)(ecol >> 5) * KBS + (u32)tok * 64 + (ecol & 31) * 2;
    u32x2 hv = ld8(rs, OFF_H + eo), xv = ld8(rs, OFF_X1 + eo);
    f32x4 acc[4][2] = {};
#pragma unroll
    for (int ks = 0; ks < 7; ++ks)
#pragma unroll
      for (int c = 0; c < 4; ++c) { acc[c][0] = mma(wf[c][ks], ff[0][ks], acc[c][0]); acc[c][1] = mma(wf[c][ks], ff[1][ks], acc[c][1]); }
    f32x4 o = reduce8(smem, acc, w, l);
    u32 a0 = pk2(o[0], o[1]), a1 = pk2(o[2], o[3]);
    u32 b0 = pk2(lo16(xv.x) + lo16(a0), hi16(xv.x) + hi16(a0)), b1 = pk2(lo16(xv.y) + lo16(a1), hi16(xv.y) + hi16(a1));
    u32x2 ov = {pk2(lo16(hv.x) + lo16(b0), hi16(hv.x) + hi16(b0)), pk2(lo16(hv.y) + lo16(b1), hi16(hv.y) + hi16(b1))};
    *(u32x2*)(A.out + (size_t)tok * 512 + ecol) = ov;
    bar();
  }
}

DI void calib(rsrc_t rs, char* ws) {
  if (threadIdx.x == 0) {
    u32 res0 = 0xFFFFFFFFu, res1 = 0xFFFFFFFFu;
    for (int rep = 0; rep < 10; ++rep) {
      u32* q = (u32*)(ws + OFF_COMM + (rep & 1) * 8192); u32 v = 0xFFFFFFFFu;
      asm volatile("global_store_dword %0, %1, off sc1\n\ts_waitcnt vmcnt(0)" :: "v"(q), "v"(v) : "memory");
      unsigned long long c1 = __builtin_amdgcn_s_memtime(); u32 r;
      asm volatile("global_load_dword %0, %1, off sc1\n\ts_waitcnt vmcnt(0)" : "=v"(r) : "v"(q) : "memory");
      unsigned long long c2 = __builtin_amdgcn_s_memtime();
      u32 d = (u32)(c2 - c1) + (r & 0);
      if (rep & 1) res1 = min(res1, d); else res0 = min(res0, d);
    }
    st4(rs, FL_SEL, res0 < res1 ? 0u : 1u);
  }
}

__global__ __launch_bounds__(512, 2) void k_mega(Args A) {
  extern __shared__ __attribute__((aligned(16))) char smem[];
  const int b = blockIdx.x, xcd = b & 7, id = (b >> 3) * 4 + (xcd & 3);
  rsrc_t rs = __builtin_amdgcn_make_buffer_rsrc((void*)A.ws, 0, WS_USED, 0x00020000);
  if (xcd < 4) {
    if (id < 32) role_X(A, smem, rs, id, xcd);
    else if (id < 96) role_Y(A, smem, rs, id - 32, xcd);
    else {
      const int a = id - 96;
      if (a == 8) calib(rs, A.ws);
      ln_task(A, rs, a);
      if (MODEV & 64) return;
      if (a < 8) role_B128<0>(A, smem, rs, a >> 2, a & 3);
      else if (a < 16) role_B128<1>(A, smem, rs, (a - 8) >> 2, a & 3);
      else role_B2c(A, smem, rs, (a - 16) >> 3, a & 7);
    }
  } else {
    if (id < 32) role_QK(A, smem, rs, id);
    else if (id < 64) role_VU(A, smem, rs, id - 32);
    else {
      fold_task(A, smem, rs, id);
      if (MODEV & 64) return;
      if (id < 96) role_T2(A, smem, rs, (id - 64) >> 3, id & 7);
      else if (id < 124) role_B128<2>(A, smem, rs, (id - 96) / 14, (id - 96) % 14);
    }
  }
}

extern "C" void kernel_launch(void* const* d_in, const int* in_sizes, int n_in, void* d_out, int out_size, void* d_ws, size_t ws_size, hipStream_t stream) {
  Args A;
  A.x = (const u16*)d_in[0]; A.mem0 = (const u16*)d_in[1];
  A.Wq = (const u16*)d_in[2]; A.Wk = (const u16*)d_in[3]; A.Wv = (const u16*)d_in[4]; A.Wo = (const u16*)d_in[5];
  A.w1 = (const u16*)d_in[8]; A.w2 = (const u16*)d_in[9]; A.w3 = (const u16*)d_in[10]; A.w4 = (const u16*)d_in[11];
  A.out = (u16*)d_out;
  char* ws = (char*)(((uintptr_t)d_ws + 2 * MiB - 1) & ~(uintptr_t)(2 * MiB - 1));
  A.ws = ws;
  hipMemsetAsync(ws, 0xFF, CTRL_BYTES, stream);
  hipFuncSetAttribute((const void*)k_mega, hipFuncAttributeMaxDynamicSharedMemorySize, LDS_BYTES);
  k_mega<<<256, 512, LDS_BYTES, stream>>>(A);
}
```

```cpp
#include <hip/hip_runtime.h>
#include <stdint.h>

typedef unsigned short u16;
typedef unsigned int u32;
typedef u32 u32x2 __attribute__((ext_vector_type(2)));
typedef u32 u32x4 __attribute__((ext_vector_type(4)));
typedef u32 u32x8 __attribute__((ext_vector_type(8)));
typedef float f32x2 __attribute__((ext_vector_type(2)));
typedef float f32x4 __attribute__((ext_vector_type(4)));
typedef short s16x4 __attribute__((ext_vector_type(4)));
typedef __bf16 bf16x2 __attribute__((ext_vector_type(2)));
typedef __bf16 bf16x8 __attribute__((ext_vector_type(8)));
typedef __bf16 bf16x16 __attribute__((ext_vector_type(16)));
typedef __attribute__((address_space(3))) s16x4* lds_s16x4_p;
#define DI __device__ __forceinline__

constexpr u32 MiB = 1u << 20;
constexpr u32 OFF_COMM = 0;
constexpr u32 OFF_FLAG = 768 * 1024;
constexpr u32 OFF_KX = 2 * MiB;
constexpr u32 OFF_QX = 18 * MiB;
constexpr u32 OFF_U  = 34 * MiB;
constexpr u32 OFF_BF = 98 * MiB;
constexpr u32 OFF_YP = 114 * MiB;
constexpr u32 OFF_XN = 130 * MiB;
constexpr u32 OFF_PT = 132 * MiB;
constexpr u32 OFF_PM = 136 * MiB;
constexpr u32 OFF_H  = 137 * MiB;
constexpr u32 OFF_YB = 139 * MiB;
constexpr u32 OFF_H1 = 141 * MiB;
constexpr u32 OFF_X1 = 143 * MiB;
constexpr u32 OFF_F  = 145 * MiB;
constexpr u32 WS_USED = 152 * MiB;
constexpr u32 KBS = 2048 * 64;
constexpr u32 FS = 128;
constexpr u32 FL_SEL = OFF_FLAG, FL_XN = FL_SEL + FS, FL_FOLD = FL_XN + 64 * FS, FL_QK = FL_FOLD + 128 * FS, FL_VU = FL_QK + 32 * FS,
              FL_XP = FL_VU + 32 * FS, FL_YE = FL_XP + 32 * FS, FL_T2 = FL_YE + 64 * FS, FL_B1 = FL_T2 + 32 * FS, FL_B2A = FL_B1 + 8 * FS,
              FL_B2B = FL_B2A + 8 * FS, FL_END = FL_B2B + 32 * FS;
constexpr u32 CTRL_BYTES = FL_END;
constexpr int SC1 = 16;
constexpr int LDS_MAIN = 144 * 1024;
constexpr int LDS_BYTES = LDS_MAIN;

DI u32 pk2(float a, float b) { f32x2 f = {a, b}; return __builtin_bit_cast(u32, __builtin_convertvector(f, bf16x2)); }
DI float lo16(u32 u) { return __uint_as_float(u << 16); }
DI float hi16(u32 u) { return __uint_as_float(u & 0xFFFF0000u); }
DI void bar() { asm volatile("" ::: "memory"); __builtin_amdgcn_s_waitcnt(0xC07F); __builtin_amdgcn_s_barrier(); asm volatile("" ::: "memory"); }
DI void drain() { asm volatile("" ::: "memory"); __builtin_amdgcn_s_waitcnt(0x0F70); asm volatile("" ::: "memory"); }
DI u32 lo8(u32 x) { return __builtin_amdgcn_update_dpp(x, x, 0x128, 0xf, 0xC, false); }
DI u32 hi8(u32 x) { return __builtin_amdgcn_update_dpp(x, x, 0x128, 0xf, 0x3, false); }
DI u32 now32() { return (u32)__builtin_amdgcn_s_memtime(); }
DI void wait_for(u32 t0, int d) { while ((int)(now32() - t0) < d) __builtin_amdgcn_s_sleep(1); }
DI float max6_red(float a, float b, float c, float d, float e, float f) {
  float t, u;
  asm("v_max3_f32 %0, %2, %3, %4\n\tv_max3_f32 %1, %5, %6, %7\n\tv_max_f32 %0, %0, %1\n\ts_nop 1\n\t"
      "v_max_f32_dpp %0, %0, %0 row_ror:8 row_mask:0xf bank_mask:0xf\n\tv_mov_b32 %1, %0\n\ts_nop 1\n\t"
      "v_permlane16_swap_b32 %0, %1\n\tv_max_f32 %0, %0, %1\n\tv_mov_b32 %1, %0\n\ts_nop 1\n\t"
      "v_permlane32_swap_b32 %0, %1\n\tv_max_f32 %0, %0, %1"
      : "=&v"(t), "=&v"(u) : "v"(a), "v"(b), "v"(c), "v"(d), "v"(e), "v"(f));
  return t;
}
DI float max3_red(float a, float b, float c) {
  float t, u;
  asm("v_max3_f32 %0, %2, %3, %4\n\ts_nop 1\n\t"
      "v_max_f32_dpp %0, %0, %0 row_ror:8 row_mask:0xf bank_mask:0xf\n\tv_mov_b32 %1, %0\n\ts_nop 1\n\t"
      "v_permlane16_swap_b32 %0, %1\n\tv_max_f32 %0, %0, %1\n\tv_mov_b32 %1, %0\n\ts_nop 1\n\t"
      "v_permlane32_swap_b32 %0, %1\n\tv_max_f32 %0, %0, %1"
      : "=&v"(t), "=&v"(u) : "v"(a), "v"(b), "v"(c));
  return t;
}
#define SB() __builtin_amdgcn_sched_barrier(0)
DI float xor8f(float x) { return __uint_as_float(__builtin_amdgcn_update_dpp(0u, __float_as_uint(x), 0x128, 0xf, 0xf, false)); }
DI float xor1f(float x) { return __uint_as_float(__builtin_amdgcn_update_dpp(0u, __float_as_uint(x), 0xB1, 0xf, 0xf, false)); }
DI float xor2f(float x) { return __uint_as_float(__builtin_amdgcn_update_dpp(0u, __float_as_uint(x), 0x4E, 0xf, 0xf, false)); }
DI float max1632(float x) {
  auto s = __builtin_amdgcn_permlane16_swap(__float_as_uint(x), __float_as_uint(x), false, false);
  x = fmaxf(__uint_as_float(s[0]), __uint_as_float(s[1]));
  auto t = __builtin_amdgcn_permlane32_swap(__float_as_uint(x), __float_as_uint(x), false, false);
  return fmaxf(__uint_as_float(t[0]), __uint_as_float(t[1]));
}
DI float sum1632(float x) {
  auto s = __builtin_amdgcn_permlane16_swap(__float_as_uint(x), __float_as_uint(x), false, false);
  x = __uint_as_float(s[0]) + __uint_as_float(s[1]);
  auto t = __builtin_amdgcn_permlane32_swap(__float_as_uint(x), __float_as_uint(x), false, false);
  return __uint_as_float(t[0]) + __uint_as_float(t[1]);
}
DI float red_max4(float x) { return max1632(fmaxf(x, xor8f(x))); }
DI float red_sum4(float x) { return sum1632(x + xor8f(x)); }
DI float red_sumq(float x) { x += xor1f(x); x += xor2f(x); return sum1632(x); }
DI f32x4 smf(u32x4 a, u32x8 b, f32x4 c, int idx) {
  return __builtin_amdgcn_smfmac_f32_16x16x64_bf16(__builtin_bit_cast(bf16x8, a), __builtin_bit_cast(bf16x16, b), c, idx, 0, 0);
}
DI f32x4 mma(u32x4 a, u32x4 b, f32x4 c) {
  return __builtin_amdgcn_mfma_f32_16x16x32_bf16(__builtin_bit_cast(bf16x8, a), __builtin_bit_cast(bf16x8, b), c, 0, 0, 0);
}
DI float gelu_t(float x) {
  float u = x * fmaf(x * x, 0.044715f * 0.7978845608028654f, 0.7978845608028654f);
  float e = __builtin_amdgcn_exp2f(u * (-2.f * 1.4426950408889634f));
  return x * __builtin_amdgcn_rcpf(1.f + e);
}
typedef __amdgpu_buffer_rsrc_t rsrc_t;
DI u32x4 ld16(rsrc_t r, u32 off) { return __builtin_amdgcn_raw_buffer_load_b128(r, off, 0, SC1); }
DI u32x2 ld8(rsrc_t r, u32 off) { return __builtin_amdgcn_raw_buffer_load_b64(r, off, 0, SC1); }
DI u32 ld4(rsrc_t r, u32 off) { return __builtin_amdgcn_raw_buffer_load_b32(r, off, 0, SC1); }
DI void st16(rsrc_t r, u32 off, u32x4 v) { __builtin_amdgcn_raw_buffer_store_b128(v, r, off, 0, SC1); }
DI void st8(rsrc_t r, u32 off, u32x2 v) { __builtin_amdgcn_raw_buffer_store_b64(v, r, off, 0, SC1); }
DI void st4(rsrc_t r, u32 off, u32 v) { __builtin_amdgcn_raw_buffer_store_b32(v, r, off, 0, SC1); }
constexpr int SPIN_MAX = 1 << 18;

struct Args {
  const u16* x; const u16* mem0; const u16* Wq; const u16* Wk; const u16* Wv; const u16* Wo;
  const u16* w1; const u16* w2; const u16* w3; const u16* w4; u16* out; char* ws;
};

DI void wait_until(unsigned long long t0, int d) {
  while ((long long)(__builtin_amdgcn_s_memtime() - t0) < (long long)d) __builtin_amdgcn_s_sleep(1);
}
DI void wait_flags(rsrc_t rs, u32 off, int n, int need, int l, int& lim, int slp = 0) {
  for (int it = 0; it < lim; ++it) {
    int v = (int)ld4(rs, off + (l < n ? l : 0) * FS);
    if (__all(v >= need)) return;
    __builtin_amdgcn_s_sleep(4);
    for (int z = 0; z < slp; ++z) __builtin_amdgcn_s_sleep(16);
  }
  lim = 1;
}
DI int wave_min(int v) {
  v = min(v, (int)__builtin_amdgcn_update_dpp(0u, (u32)v, 0xB1, 0xf, 0xf, false));
  v = min(v, (int)__builtin_amdgcn_update_dpp(0u, (u32)v, 0x4E, 0xf, 0xf, false));
  v = min(v, (int)__builtin_amdgcn_update_dpp(0u, (u32)v, 0x141, 0xf, 0xf, false));
  v = min(v, (int)__builtin_amdgcn_update_dpp(0u, (u32)v, 0x140, 0xf, 0xf, false));
  auto s = __builtin_amdgcn_permlane16_swap((u32)v, (u32)v, false, false);
  v = min((int)s[0], (int)s[1]);
  auto t = __builtin_amdgcn_permlane32_swap((u32)v, (u32)v, false, false);
  v = min((int)t[0], (int)t[1]);
  return __builtin_amdgcn_readfirstlane(v);
}
DI void wait_flags_bk(rsrc_t rs, u32 off, int n, int need, int l, int& lim) {
  for (int it = 0; it < lim; ++it) {
    int v = (int)ld4(rs, off + (l < n ? l : 0) * FS);
    if (__all(v >= need)) return;
    int d = need - wave_min(v);
    __builtin_amdgcn_s_sleep(4);
    for (int z = 1; z < min(d, 8); ++z) __builtin_amdgcn_s_sleep(48);
  }
  lim = 1;
}
DI void wait_prog(rsrc_t rs, u32 off, int n, int need, int l, int& cache, int& lim) {
  if (cache >= need) return;
  for (int it = 0; it < lim; ++it) {
    int v = (int)ld4(rs, off + (l < n ? l : 0) * FS);
    if (__all(v >= need)) { cache = wave_min(v); return; }
    __builtin_amdgcn_s_sleep(2);
  }
  lim = 1;
}
DI u32x4 poll_mem(rsrc_t rs, u32 off, int& lim) {
  u32x4 m = {0xFFFFFFFFu, 0, 0, 0};
  bool ok = false;
  int it = 0;
  for (; it < lim; ++it) {
    if (!ok) m = ld16(rs, off);
    u32 mx = max(max(m.x, m.y), max(m.z, m.w));
    ok = mx != 0xFFFFFFFFu;
    if (__all(ok)) break;
    asm volatile("" ::: "memory");
  }
  if (it >= lim) lim = 1;
  return m;
}

DI bool mem_ok(const u32x4& m) { return __all(max(max(m.x, m.y), max(m.z, m.w)) != 0xFFFFFFFFu); }
#define MEMF() asm volatile("" ::: "memory")
template <int GAP>
DI void poll_mem3(rsrc_t rs, u32 off, u32 ldsAddr, int& lim, u32x4& pa, u32x4& pb, u32x4& pc, int& itc) {
  u32 t; int it;
#define PLD(R) "buffer_load_dwordx4 " R ", %[off], %[rs], 0 offen sc1\n\t"
#define PCK(x, y, z, w_, L) "s_waitcnt vmcnt(2)\n\tv_max_u32 %[t], " z ", " w_ "\n\tv_max3_u32 %[t], " x ", " y ", %[t]\n\tv_cmp_ne_u32 vcc, -1, %[t]\n\ts_cmp_eq_u64 vcc, exec\n\ts_cbranch_scc1 " L "\n\t"
  asm volatile(
      PLD("v[240:243]") "s_sleep %[gap]\n\t" PLD("v[244:247]") "s_sleep %[gap]\n\t" PLD("v[248:251]")
      "s_mov_b32 %[it], 0\n"
      "1:\n\t"
      PCK("v240", "v241", "v242", "v243", "2f") PLD("v[240:243]")
      PCK("v244", "v245", "v246", "v247", "3f") PLD("v[244:247]")
      PCK("v248", "v249", "v250", "v251", "4f") PLD("v[248:251]")
      "s_add_i32 %[it], %[it], 1\n\ts_cmp_lt_i32 %[it], %[lim]\n\ts_cbranch_scc1 1b\n\t"
      "s_waitcnt vmcnt(0)\n"
      "4:\n\tds_write_b128 %[lds], v[248:251]\n\ts_branch 5f\n"
      "3:\n\tds_write_b128 %[lds], v[244:247]\n\ts_branch 5f\n"
      "2:\n\tds_write_b128 %[lds], v[240:243]\n"
      "5:"
      : "={v[240:243]}"(pa), "={v[244:247]}"(pb), "={v[248:251]}"(pc), [t] "=&v"(t), [it] "=&s"(it)
      : [off] "v"(off), [rs] "s"(rs), [lim] "s"(lim), [lds] "v"(ldsAddr), [gap] "n"(GAP)
      : "vcc", "scc", "memory");
#undef PLD
#undef PCK
  if (it >= lim) lim = 1;
  itc = it;
}
template <int N>
DI void poll_retire(u32x4& pa, u32x4& pb, u32x4& pc) {
  asm volatile("s_waitcnt vmcnt(%3)" :: "{v[240:243]}"(pa), "{v[244:247]}"(pb), "{v[248:251]}"(pc), "n"(N));
}

template <bool KPERM>
DI u32x4 frag_tr(const char* L, int stride, int row0, int cb, int l) {
  const int g = l >> 4, q = (l & 15) >> 2;
  const int ra = row0 + (KPERM ? 4 * g : 8 * g) + q, rb = ra + (KPERM ? 16 : 4);
  u32x2 a = __builtin_bit_cast(u32x2, __builtin_amdgcn_ds_read_tr16_b64_v4i16((lds_s16x4_p)(L + ra * stride + cb)));
  u32x2 b = __builtin_bit_cast(u32x2, __builtin_amdgcn_ds_read_tr16_b64_v4i16((lds_s16x4_p)(L + rb * stride + cb)));
  return u32x4{a.x, a.y, b.x, b.y};
}
template <int PPR, int NP>
DI void slab_load(const u16* W, int ld, int t, u32x4 (&tmp)[NP]) {
#pragma unroll
  for (int i = 0; i < NP; ++i) { int id = i * 512 + t; tmp[i] = *(const u32x4*)(W + (size_t)(id / PPR) * ld + (id % PPR) * 8); }
}
template <int PPR, int NP>
DI void slab_store(char* L, int t, const u32x4 (&tmp)[NP]) {
#pragma unroll
  for (int i = 0; i < NP; ++i) { int id = i * 512 + t; *(u32x4*)(L + (id / PPR) * (PPR * 16 + 16) + (id % PPR) * 16) = tmp[i]; }
}

DI void ln_center(const u32x4 (&v)[4], f32x2 (&c)[16], float& var) {
  f32x2 s2 = {0.f, 0.f};
#pragma unroll
  for (int i = 0; i < 4; ++i)
#pragma unroll
    for (int k = 0; k < 4; ++k) { c[i * 4 + k] = f32x2{lo16(v[i][k]), hi16(v[i][k])}; s2 += c[i * 4 + k]; }
  float mean = red_sumq(s2.x + s2.y) * (1.f / 512.f);
  f32x2 m2 = {mean, mean}, q2 = {0.f, 0.f};
#pragma unroll
  for (int i = 0; i < 16; ++i) { c[i] -= m2; q2 += c[i] * c[i]; }
  var = red_sumq(q2.x + q2.y) * (1.f / 512.f);
}
DI u32x4 ln_pack(const f32x2 (&c)[16], int i, float r) {
  f32x2 r2 = {r, r};
  u32x4 o;
#pragma unroll
  for (int k = 0; k < 4; ++k) { f32x2 z = c[i * 4 + k] * r2; o[k] = pk2(z.x, z.y); }
  return o;
}

template <class F>
DI void project(const u32x8 (&wf)[2][8], const char* memL, int l, int idx, float (&v)[4], F&& extra) {
  f32x4 a0 = {0, 0, 0, 0}, a1 = {0, 0, 0, 0};
  u32x4 fr[8];
#pragma unroll
  for (int i = 0; i < 8; ++i) fr[i] = *(const u32x4*)(memL + (i << 10) + l * 16);
  __builtin_amdgcn_sched_barrier(0);
  extra();
  __builtin_amdgcn_sched_barrier(0);
#pragma unroll
  for (int i = 0; i < 8; ++i) { a0 = smf(fr[i], wf[0][i], a0, idx); a1 = smf(fr[i], wf[1][i], a1, idx); }
#pragma unroll
  for (int r = 0; r < 4; ++r) {
    auto s = __builtin_amdgcn_permlane32_swap(__float_as_uint(a0[r]), __float_as_uint(a1[r]), false, false);
    v[r] = __uint_as_float(s[0]) + __uint_as_float(s[1]);
  }
}

#define XGAP 3
#define SLPV 0
#define MODEV 0
#define DXV 0
#define DYV 1800
#define DYMV 800
#define YREP 4
#define ADEC 16
#define AINC 128

DI void role_X(const Args& A, char* smem, rsrc_t rs, int h, int rep) {
  const int t = threadIdx.x, l = t & 63, w = __builtin_amdgcn_readfirstlane(t >> 6);
  const int g = l >> 4;
  int lim = SPIN_MAX;
  u32x8 wf[2][8];
  {
    char* st = smem + w * (128 * 80);
    const u16* W = (w < 4 ? A.Wq : A.Wk) + h * 128 + (w & 3) * 32;
#pragma unroll
    for (int c = 0; c < 4; ++c) {
      u32x4 tmp[8];
#pragma unroll
      for (int i = 0; i < 8; ++i) {
        int row = (l >> 2) + 16 * i;
        int kc = 2 * c + (row >> 6);
        tmp[i] = *(const u32x4*)(W + (size_t)(kc * 64 + (row & 63)) * 4096 + (l & 3) * 8);
      }
#pragma unroll
      for (int i = 0; i < 8; ++i) *(u32x4*)(st + ((l >> 2) + 16 * i) * 80 + (l & 3) * 16) = tmp[i];
      asm volatile("s_waitcnt lgkmcnt(0)" ::: "memory");
      const int q = (l & 15) >> 2, p = l & 3;
      const int rbase = 16 * (g >> 1) + 4 * (g & 1) + (q & 1) + 8 * (q >> 1);
#pragma unroll
      for (int sl = 0; sl < 2; ++sl)
#pragma unroll
        for (int tt = 0; tt < 2; ++tt)
#pragma unroll
          for (int u = 0; u < 4; ++u) {
            int row = sl * 64 + rbase + 32 * (u >> 1) + 2 * (u & 1);
            s16x4 r = __builtin_amdgcn_ds_read_tr16_b64_v4i16((lds_s16x4_p)(st + row * 80 + (tt * 16 + p * 4) * 2));
            u32x2 r2 = __builtin_bit_cast(u32x2, r);
            wf[tt][2 * c + sl][2 * u] = r2.x; wf[tt][2 * c + sl][2 * u + 1] = r2.y;
          }
      asm volatile("s_waitcnt lgkmcnt(0)" ::: "memory");
    }
  }
  wait_flags(rs, FL_SEL, 1, 0, l, lim);
  u32 sel = __builtin_amdgcn_readfirstlane(ld4(rs, FL_SEL)) & 1;
  if (MODEV & 1) sel = 0;
  if (MODEV & 2) sel = 1;
  __syncthreads();
  char* memL = smem;
  char* qkL = smem + 8192;
  const int idx = (l & 8) ? 0xEEEE : 0x4444;
  const u32 commB = OFF_COMM + sel * 8192;
  const u32 memOff = commB + ((MODEV & 1024) ? 0 : (MODEV & 2048) ? (rep & 1) : (MODEV & 4096) ? (rep + 4 * ((h >> 2) & 1)) : rep) * 16384 + w * 1024 + l * 16;
  const u32 pOff = commB + (32 + (h >> 3)) * 16384;
  const int a = (l >> 3) & 1, r8 = l & 7;
  const u32 pTok = pOff + (h & 7) * 512 + (g * 8 + r8) * 16 + a * 65536;
  const u32 pMM = pOff + 4096 + (h & 7) * 128 + r8 * 16 + ((g & 1) * 2 + a) * 65536;
  const float csc = 0.08838834764831845f * 1.4426950408889634f;
  const bool isT1 = (w == 1 || w == 2);
  const int tt1 = w - 1;
  char* KQL = smem + 16384;
  const u32 kqOff = (w < 6 ? OFF_KX : OFF_QX) + (u32)h * 8192 + ((w & 1) * 16 + (l & 15)) * 256 + g * 16;
  int qkdone = -1;
  u32x4 aux[8];
#define kq aux
#pragma unroll
  for (int i = 0; i < 8; ++i) aux[i] = u32x4{0, 0, 0, 0};
  if (w >= 4) {
    wait_prog(rs, FL_QK + h * FS, 1, 0, l, qkdone, lim);
#pragma unroll
    for (int i = 0; i < 4; ++i) kq[i] = ld16(rs, kqOff + i * 64);
  }
  const u32 ptOff = OFF_PT + (u32)(h * 2 + (tt1 & 1)) * 1024 + l * 16;
  const u32 pmOff = OFF_PM + (u32)((h >> 2) * 2 + (tt1 & 1)) * 1024 + (h & 3) * 256 + (l & 15) * 16 + (g & 1) * 8;
  if (w >= 4) {
#pragma unroll
    for (int i = 0; i < 4; ++i) *(u32x4*)(KQL + ((w - 4) * 4 + i) * 1024 + l * 16) = kq[i];
  }
  bar();
  if (w == 0) {
#pragma unroll
    for (int tt = 0; tt < 2; ++tt)
#pragma unroll
      for (int ks = 0; ks < 4; ++ks) aux[tt * 4 + ks] = *(const u32x4*)(KQL + (tt * 4 + ks) * 1024 + l * 16);
  }
  if (w >= 4) wait_prog(rs, FL_QK + h * FS, 1, 1, l, qkdone, lim);
  u32x4 m = *(const u32x4*)(A.mem0 + (l & 7) * 512 + w * 64 + (l >> 3) * 8);
  u32x4 pa = m, pb = m, pc = m;
  int itc = 0;
  *(u32x4*)(memL + w * 1024 + l * 16) = m;
  const u32 memLds = (u32)(size_t)(__attribute__((address_space(3))) char*)(memL + w * 1024 + l * 16);
  for (int s = 0; s < 64; ++s) {
    char* kqb = KQL + (s & 1) * 16384;
    bar();
    float v[4];
    project(wf, memL, l, idx, v, [&]() {
      if (w >= 4 && s < 63 && !(MODEV & 32)) {
#pragma unroll
        for (int i = 0; i < 4; ++i) kq[i] = ld16(rs, kqOff + (u32)(s + 1) * (32 * 8192) + i * 64);
      }
    });
    {
      *(u32x2*)(qkL + (w * 32 + (g >> 1) * 16 + (l & 15)) * 16 + (g & 1) * 8) = u32x2{pk2(v[0], v[1]), pk2(v[2], v[3])};
    }
    if (isT1) drain();
    bar();
    if (w >= 4 && s < 63 && !(MODEV & 32)) poll_retire<4>(pa, pb, pc); else poll_retire<0>(pa, pb, pc);
    if (isT1 && s > 0 && !(MODEV & 8)) {
      st16(rs, ptOff + (u32)(s - 1) * 65536, aux[0]);
      if (g < 2) st8(rs, pmOff + (u32)(s - 1) * 16384, u32x2{aux[1].x, aux[1].y});
    }
    if (w == 3 && s >= 2 && l == 0 && !(MODEV & 256)) st4(rs, FL_XP + h * FS, (u32)(s - 2));
    if (w == 0 && s < 63) {
      u32x4 qf[4], mf[4];
#pragma unroll
      for (int ks = 0; ks < 4; ++ks) { qf[ks] = frag_tr<false>(qkL, 16, ks * 32, (l & 1) * 8, l); SB(); }
#pragma unroll
      for (int ks = 0; ks < 4; ++ks) { mf[ks] = frag_tr<false>(qkL + 2048, 16, ks * 32, (l & 1) * 8, l); SB(); }
      float ci = g >= 2 ? -1e30f : 0.f;
      f32x4 d0 = {ci, ci, ci, ci}, d1 = {0, 0, 0, 0}, d2 = {0, 0, 0, 0};
#pragma unroll
      for (int ks = 0; ks < 4; ++ks) { d1 = mma(aux[ks], qf[ks], d1); d2 = mma(aux[4 + ks], qf[ks], d2); SB(); }
      d0 = mma(mf[0], qf[0], d0); d0 = mma(mf[1], qf[1], d0); SB();
      float e0 = a ? d2[0] : d1[0], e1 = a ? d2[1] : d1[1], e2 = a ? d2[2] : d1[2], e3 = a ? d2[3] : d1[3];
      float m4 = fmaxf(fmaxf(e0, e1), fmaxf(e2, e3));
      SB();
      d0 = mma(mf[2], qf[2], d0); d0 = mma(mf[3], qf[3], d0); SB();
      float e4 = a ? d0[2] : d0[0], e5 = a ? d0[3] : d0[1];
      float mx = max3_red(m4, e4, e5);
      float mc = -mx * csc;
      e0 = __builtin_amdgcn_exp2f(fmaf(e0, csc, mc)); e1 = __builtin_amdgcn_exp2f(fmaf(e1, csc, mc));
      e2 = __builtin_amdgcn_exp2f(fmaf(e2, csc, mc)); e3 = __builtin_amdgcn_exp2f(fmaf(e3, csc, mc));
      e4 = __builtin_amdgcn_exp2f(fmaf(e4, csc, mc)); e5 = __builtin_amdgcn_exp2f(fmaf(e5, csc, mc));
      float sum = ((e0 + e1) + (e2 + e3)) + (e4 + e5);
      sum = red_sum4(sum);
      float rl = __builtin_amdgcn_rcpf(sum);
      rl = (s & 1) ? -rl : rl;
      u32x2 pt = {pk2(e0 * rl, e1 * rl), pk2(e2 * rl, e3 * rl)};
      u32 pm = pk2(e4 * rl, e5 * rl);
      const u32x4 gt = {lo8(pt.x), lo8(pt.y), hi8(pt.x), hi8(pt.y)};
      st16(rs, pTok, gt); st16(rs, pTok + 2 * 65536, gt);
      const u32 ml = lo8(pm), mh = hi8(pm);
      auto sl = __builtin_amdgcn_permlane16_swap(ml, ml, false, false);
      auto sh = __builtin_amdgcn_permlane16_swap(mh, mh, false, false);
      if (l < 32) st16(rs, pMM, u32x4{sl[0], sh[0], sl[1], sh[1]});
    }
    if (isT1 && !(MODEV & 8)) {
      __builtin_amdgcn_s_sleep(3);
      u32x4 mf[4], kt[2][4], qt[4];
#pragma unroll
      for (int ks = 0; ks < 4; ++ks) {
        qt[ks] = *(const u32x4*)(kqb + (8 + tt1 * 4 + ks) * 1024 + l * 16);
        kt[0][ks] = *(const u32x4*)(kqb + ks * 1024 + l * 16); kt[1][ks] = *(const u32x4*)(kqb + (4 + ks) * 1024 + l * 16);
        mf[ks] = frag_tr<false>(qkL + 2048, 16, ks * 32, (l & 1) * 8, l);
      }
      f32x4 d0 = {0, 0, 0, 0}, d1 = {0, 0, 0, 0}, d2 = {0, 0, 0, 0};
#pragma unroll
      for (int ks = 0; ks < 4; ++ks) { d1 = mma(kt[0][ks], qt[ks], d1); d2 = mma(kt[1][ks], qt[ks], d2); }
#pragma unroll
      for (int ks = 0; ks < 4; ++ks) d0 = mma(mf[ks], qt[ks], d0);
      const int qi = tt1 * 16 + (l & 15);
      float e[12];
#pragma unroll
      for (int r = 0; r < 4; ++r) {
        e[r] = (4 * g + r <= qi) ? d1[r] : -1e30f;
        e[4 + r] = (16 + 4 * g + r <= qi) ? d2[r] : -1e30f;
        e[8 + r] = (g < 2) ? d0[r] : -1e30f;
      }
      float mx = e[0];
#pragma unroll
      for (int r = 1; r < 12; ++r) mx = fmaxf(mx, e[r]);
      mx = max1632(mx);
      float mc = -mx * csc, sum = 0.f;
#pragma unroll
      for (int r = 0; r < 12; ++r) { e[r] = __builtin_amdgcn_exp2f(fmaf(e[r], csc, mc)); sum += e[r]; }
      sum = sum1632(sum);
      float rl = __builtin_amdgcn_rcpf(sum);
      aux[0] = u32x4{pk2(e[0] * rl, e[1] * rl), pk2(e[2] * rl, e[3] * rl), pk2(e[4] * rl, e[5] * rl), pk2(e[6] * rl, e[7] * rl)};
      aux[1].x = pk2(e[8] * rl, e[9] * rl); aux[1].y = pk2(e[10] * rl, e[11] * rl);
    }
    if (s < 63) {
      char* kqn = KQL + ((s + 1) & 1) * 16384;
      if (w >= 4) {
        __builtin_amdgcn_s_sleep(4);
        wait_prog(rs, FL_QK + h * FS, 1, min(s + 2, 63), l, qkdone, lim);
#pragma unroll
        for (int i = 0; i < 4; ++i) *(u32x4*)(kqn + ((w - 4) * 4 + i) * 1024 + l * 16) = kq[i];
      }
      bar();
      if (w == 0) {
#pragma unroll
        for (int tt = 0; tt < 2; ++tt)
#pragma unroll
          for (int ks = 0; ks < 4; ++ks) aux[tt * 4 + ks] = *(const u32x4*)(kqn + (tt * 4 + ks) * 1024 + l * 16);
      }
      poll_mem3<XGAP>(rs, memOff + ((s + 1) & 3) * 131072, memLds, lim, pa, pb, pc, itc);
    }
  }
  if (isT1) {
    st16(rs, ptOff + 63u * 65536, aux[0]);
    if (g < 2) st8(rs, pmOff + 63u * 16384, u32x2{aux[1].x, aux[1].y});
  }
  drain();
  bar();
  if (t == 192) st4(rs, FL_XP + h * FS, 63u);
}

#undef kq
DI void fold_task(const Args& A, char* smem, rsrc_t rs, int task) {
  const int t = threadIdx.x, l = t & 63, w = __builtin_amdgcn_readfirstlane(t >> 6);
  const int g = l >> 4, h = task >> 2, q = task & 3;
  const int r = l & 15, gr = r >> 2, rg = r & 3;
  const int drow = 64 * w + 16 * (gr >> 1) + 4 * (gr & 1) + 8 * (rg >> 1) + (rg & 1);
  const u16* Wv = A.Wv + (size_t)drow * 4096 + h * 128 + g * 8;
  u32x4 tmp[4], af[4][4];
  slab_load<16, 4>(A.Wo + (size_t)h * 128 * 512 + 128 * q, 512, t, tmp);
#pragma unroll
  for (int u = 0; u < 4; ++u)
#pragma unroll
    for (int ks = 0; ks < 4; ++ks) af[u][ks] = *(const u32x4*)(Wv + (size_t)(32 * (u >> 1) + 2 * (u & 1)) * 4096 + ks * 32);
  __builtin_amdgcn_sched_barrier(0);
  slab_store<16, 4>(smem, t, tmp);
  __syncthreads();
#pragma unroll
  for (int ct = 0; ct < 8; ++ct) {
    u32x4 wo[4];
#pragma unroll
    for (int ks = 0; ks < 4; ++ks) wo[ks] = frag_tr<false>(smem, 272, ks * 32, (16 * ct + 4 * (l & 3)) * 2, l);
    const int c = 128 * q + 16 * ct + r, cc = c >> 3, jj = c & 7;
#pragma unroll
    for (int half = 0; half < 2; ++half) {
      f32x4 a0 = {0, 0, 0, 0}, a1 = {0, 0, 0, 0};
#pragma unroll
      for (int ks = 0; ks < 4; ++ks) { a0 = mma(af[2 * half][ks], wo[ks], a0); a1 = mma(af[2 * half + 1][ks], wo[ks], a1); }
      const u32 o = (u32)((((((cc * 8 + (h >> 2)) * 2 + ((h >> 1) & 1)) * 8 + w) * 2 + half) * 64 + g * 16 + (h & 1) * 8 + jj)) * 16;
      st16(rs, OFF_BF + o, u32x4{pk2(a0[0], a0[1]), pk2(a0[2], a0[3]), pk2(a1[0], a1[1]), pk2(a1[2], a1[3])});
    }
  }
  drain();
  __syncthreads();
  if (t == 0) st4(rs, FL_FOLD + task * FS, 1u);
}

DI void ln_task(const Args& A, rsrc_t rs, int s0) {
  const int t = threadIdx.x, l = t & 63, w = __builtin_amdgcn_readfirstlane(t >> 6);
  const int g = l & 3, tk = (l >> 2) & 3, kq = l >> 4, tok = 4 * w + tk;
  u32x4 v[2][4];
#pragma unroll
  for (int u = 0; u < 2; ++u)
#pragma unroll
    for (int i = 0; i < 4; ++i) v[u][i] = *(const u32x4*)(A.x + (size_t)(32 * (s0 + 32 * u) + tok) * 512 + 32 * (4 * i + kq) + 8 * g);
#pragma unroll
  for (int u = 0; u < 2; ++u) {
    f32x2 c[16]; float var;
    ln_center(v[u], c, var);
    float r = __builtin_amdgcn_rsqf(var + 1e-5f);
#pragma unroll
    for (int i = 0; i < 4; ++i)
      st16(rs, OFF_XN + (u32)(s0 + 32 * u) * 32768 + (u32)((((tok >> 4) * 16 + 4 * i + kq) * 64) + g * 16 + ((tok & 15) ^ (4 * g))) * 16, ln_pack(c, i, r));
  }
  drain();
  __syncthreads();
  if (t < 2) st4(rs, FL_XN + (s0 + 32 * t) * FS, 1u);
}

DI void role_Y(const Args& A, char* smem, rsrc_t rs, int cc, int rep) {
  const int t = threadIdx.x, l = t & 63, w = __builtin_amdgcn_readfirstlane(t >> 6);
  const int g = l >> 4;
  int lim = SPIN_MAX;
  fold_task(A, smem, rs, cc);
  wait_flags(rs, FL_FOLD, 64, 0, l, lim);
  wait_flags(rs, FL_FOLD + 64 * FS, 64, 0, l, lim);
  u32x8 wf[2][8];
#pragma unroll
  for (int tt = 0; tt < 2; ++tt)
#pragma unroll
    for (int i = 0; i < 8; ++i) {
      int kc = i;
      u32 o = OFF_BF + (u32)((((cc * 8 + w) * 2 + tt) * 8 + kc) * 2) * 1024 + l * 16;
      u32x4 lo = ld16(rs, o), hi = ld16(rs, o + 1024);
      wf[tt][i] = u32x8{lo.x, lo.y, lo.z, lo.w, hi.x, hi.y, hi.z, hi.w};
    }
  wait_flags(rs, FL_SEL, 1, 0, l, lim);
  u32 sel = __builtin_amdgcn_readfirstlane(ld4(rs, FL_SEL)) & 1;
  if (MODEV & 1) sel = 0;
  if (MODEV & 2) sel = 1;
  char* memL = smem;
  char* YL = smem + 8192;
  float* RL = (float*)(smem + 12288);
  const int idx = (l & 8) ? 0xEEEE : 0x4444;
  const u32 commB = OFF_COMM + sel * 8192;
  const u32 memOff = commB + (rep + YREP) * 16384 + w * 1024 + l * 16;
  const int set = (l >> 3) & 1, r8 = l & 7;
  const u32 pBase = commB + (32 + rep * 4 + (w & 3)) * 16384;
  const u32 pTok = pBase + set * 512 + (g * 8 + r8) * 16;
  const u32 pMM = pBase + 4096 + (set * 4 + g) * 128 + r8 * 16;
  const u32 uOff = OFF_U + (u32)cc * 16384 + (u32)(8 * (w & 3) + set) * 512 + g * 128 + r8 * 16;
  char* UL = smem + 16384;
  int vudone = -1;
  u32x4 un[4];
  if (w >= 4) {
    wait_prog(rs, FL_VU, 32, 0, l, vudone, lim);
#pragma unroll
    for (int mm = 0; mm < 4; ++mm) un[mm] = ld16(rs, uOff + mm * 1024);
  }
  u32x4 m = *(const u32x4*)(A.mem0 + (l & 7) * 512 + w * 64 + (l >> 3) * 8);
  const int rlIdx = (l & 7) * 8 + ((l >> 4) & 1) + 2 * (l >> 5) + 4 * ((l >> 3) & 1);
  const u32 pubOff = commB + (l >> 3) * 16384 + cc * 128 + (l & 7) * 16;
  u32 tref = now32();
  int dP = DYV;
  for (int s = 0; s < 64; ++s) {
    *(u32x4*)(memL + w * 1024 + l * 16) = m;
    char* ub = UL + (s & 1) * 16384 + (w & 3) * 4096 + l * 16;
    if (w >= 4) {
#pragma unroll
      for (int mm = 0; mm < 4; ++mm) *(u32x4*)(ub + mm * 1024) = un[mm];
    }
    bar();
    if (w >= 4 && s < 62 && !(MODEV & 16)) {
      wait_prog(rs, FL_VU, 32, s + 1, l, vudone, lim);
#pragma unroll
      for (int mm = 0; mm < 4; ++mm) un[mm] = ld16(rs, uOff + (u32)(s + 1) * (64 * 16384) + mm * 1024);
    }
    if (w == 5 && s >= 2)
      st16(rs, commB + (((s - 1) & 3) * 8 + (l >> 3)) * 16384 + cc * 128 + (l & 7) * 16, u32x4{0xFFFFFFFFu, 0xFFFFFFFFu, 0xFFFFFFFFu, 0xFFFFFFFFu});
    float v[4];
    u32x4 uf[4];
    project(wf, memL, l, idx, v, [&]() {
      if (w < 4) {
#pragma unroll
        for (int mm = 0; mm < 4; ++mm) uf[mm] = *(const u32x4*)(ub + mm * 1024);
      }
    });
    {
      u32x2 pk = {pk2(v[0], v[1]), pk2(v[2], v[3])};
      int head = 4 * w + 2 * (g >> 1) + ((l >> 3) & 1);
      *(u32x2*)(YL + (head * 8 + (l & 7)) * 16 + (g & 1) * 8) = pk;
    }
    bar();
    if (w >= 5 && s < 61) wait_prog(rs, FL_VU, 32, s + 2, l, vudone, lim);
    if (w == 4 && !(MODEV & 128)) {
#pragma unroll
      for (int i = 0; i < 4; ++i) {
        int id = i * 64 + l;
        u32x4 y = *(const u32x4*)(YL + id * 16);
        st16(rs, OFF_YP + (u32)((s * 32 + (id >> 3)) * 512 + cc * 8 + (id & 7)) * 16, y);
      }
    }
    if (w == 4 && s < 61) wait_prog(rs, FL_VU, 32, s + 2, l, vudone, lim);
    if (s < 63) {
      if (w < 4) {
        u32x4 yf = *(const u32x4*)(YL + ((8 * w + 4 * set + g) * 8 + r8) * 16);
        u32x4 pf[5];
        wait_for(tref, dP);
        const u32 tm = (s & 1) ? 0x80008000u : 0u;
        bool ok0 = false, ok1 = false, ok2 = false, ok3 = false, ok4 = false;
        int it = 0;
        for (; it < lim; ++it) {
          if (!ok0) pf[0] = ld16(rs, pTok);
          if (!ok1) pf[1] = ld16(rs, pTok + 1024);
          if (!ok2) pf[2] = ld16(rs, pTok + 2048);
          if (!ok3) pf[3] = ld16(rs, pTok + 3072);
          if (!ok4) pf[4] = ld16(rs, pMM);
          ok0 = (((pf[0].x ^ tm) | (pf[0].z ^ tm)) & 0x80008000u) == 0;
          ok1 = (((pf[1].x ^ tm) | (pf[1].z ^ tm)) & 0x80008000u) == 0;
          ok2 = (((pf[2].x ^ tm) | (pf[2].z ^ tm)) & 0x80008000u) == 0;
          ok3 = (((pf[3].x ^ tm) | (pf[3].z ^ tm)) & 0x80008000u) == 0;
          ok4 = (((pf[4].x ^ tm) | (pf[4].y ^ tm) | (pf[4].z ^ tm) | (pf[4].w ^ tm)) & 0x80008000u) == 0;
          if (__all(ok0 && ok1 && ok2 && ok3 && ok4)) break;
          asm volatile("" ::: "memory");
        }
        if (it >= lim) lim = 1;
        dP += (it == 0) ? -ADEC : min(it, 4) * AINC;
        dP = max(min(dP, 6000), 0);
        f32x4 d = {0, 0, 0, 0}, d2 = {0, 0, 0, 0};
        d = mma(pf[0], uf[0], d); d2 = mma(pf[1], uf[1], d2); d = mma(pf[2], uf[2], d); d2 = mma(pf[3], uf[3], d2); d = mma(pf[4], yf, d);
        d += d2;
        if ((g >> 1) == set) {
          float* dst = RL + (w * 2 + set) * 64 + (4 * (g & 1)) * 8 + r8;
          dst[0] = d[0]; dst[8] = d[1]; dst[16] = d[2]; dst[24] = d[3];
        }
      }
      bar();
      if (w == 0) {
        const float* R = RL + rlIdx;
        float x0 = R[0], x1 = R[64], x2 = R[128], x3 = R[192], x4 = R[256], x5 = R[320], x6 = R[384], x7 = R[448];
        float sum = ((x0 + x1) + (x2 + x3)) + ((x4 + x5) + (x6 + x7));
        auto s16 = __builtin_amdgcn_permlane16_swap(__float_as_uint(sum), __float_as_uint(sum), false, false);
        u32 pk = pk2(__uint_as_float(s16[0]), __uint_as_float(s16[1]));
        if (s & 1) pk ^= 0x80008000u;
        pk = min(pk, 0xFFFFFFFEu);
        auto s32 = __builtin_amdgcn_permlane32_swap(pk, pk, false, false);
        u32x4 o = {lo8(s32[0]), lo8(s32[1]), hi8(s32[0]), hi8(s32[1])};
        if (l < 32 || YREP) st16(rs, pubOff + ((s + 1) & 3) * 131072, o);
      }
      const u32 tpub = now32();
      wait_for(tpub, DYMV);
      m = poll_mem(rs, memOff + ((s + 1) & 3) * 131072, lim);
      tref = now32();
      drain();
    }
  }
  drain();
  bar();
  if (t == 0) st4(rs, FL_YE + cc * FS, 1u);
}

DI void role_QK(const Args& A, char* smem, rsrc_t rs, int h) {
  const int t = threadIdx.x, l = t & 63, w = __builtin_amdgcn_readfirstlane(t >> 6);
  const int g = l >> 4;
  int lim = SPIN_MAX;
  u32x4 wf[2][16];
#pragma unroll
  for (int rnd = 0; rnd < 2; ++rnd) {
    u32x4 tmp[16];
#pragma unroll
    for (int i = 0; i < 16; ++i) {
      int id = i * 512 + t, row = id >> 5, c8 = id & 31;
      tmp[i] = *(const u32x4*)((c8 < 16 ? A.Wq : A.Wk) + (size_t)(rnd * 256 + row) * 4096 + h * 128 + (c8 & 15) * 8);
    }
    slab_store<32, 16>(smem, t, tmp);
    __syncthreads();
#pragma unroll
    for (int ks = 0; ks < 8; ++ks)
#pragma unroll
      for (int te = 0; te < 2; ++te) wf[te][rnd * 8 + ks] = frag_tr<false>(smem, 528, ks * 32, (32 * w + 8 * (l & 3) + 4 * te) * 2, l);
    __syncthreads();
  }
  if (w == 0) wait_flags(rs, FL_XN, 8, 0, l, lim);
  __syncthreads();
  u32x4 pf[4];
#pragma unroll
  for (int i = 0; i < 4; ++i) pf[i] = ld16(rs, OFF_XN + (i * 512 + t) * 16);
#pragma unroll
  for (int i = 0; i < 4; ++i) *(u32x4*)(smem + (i * 512 + t) * 16) = pf[i];
  bar();
  const int slot = (g * 16 + ((l & 15) ^ (4 * g))) * 16;
  const u32 oOff = (w < 4 ? OFF_QX : OFF_KX) + (u32)h * 8192 + (l & 15) * 256 + ((w & 3) * 32 + 8 * g) * 2;
  for (int s = 0; s < 64; ++s) {
    if (s == 6) { if (w == 0) wait_flags(rs, FL_XN, 64, 0, l, lim); bar(); }
#pragma unroll
    for (int i = 0; i < 4; ++i) pf[i] = ld16(rs, OFF_XN + (u32)min(s + 1, 63) * 32768 + (i * 512 + t) * 16);
    __builtin_amdgcn_sched_barrier(0);
    const char* xb = smem + (s & 1) * 32768 + slot;
    f32x4 acc[2][2] = {};
    u32x4 bb[2][4][2];
#pragma unroll
    for (int k = 0; k < 4; ++k) { bb[0][k][0] = *(const u32x4*)(xb + k * 1024); bb[0][k][1] = *(const u32x4*)(xb + 16384 + k * 1024); }
#pragma unroll
    for (int gq = 0; gq < 4; ++gq) {
      if (gq < 3) {
#pragma unroll
        for (int k = 0; k < 4; ++k) {
          bb[(gq + 1) & 1][k][0] = *(const u32x4*)(xb + ((gq + 1) * 4 + k) * 1024);
          bb[(gq + 1) & 1][k][1] = *(const u32x4*)(xb + 16384 + ((gq + 1) * 4 + k) * 1024);
        }
      }
      __builtin_amdgcn_sched_barrier(0);
#pragma unroll
      for (int k = 0; k < 4; ++k) {
        const int ks = gq * 4 + k;
        acc[0][0] = mma(wf[0][ks], bb[gq & 1][k][0], acc[0][0]); acc[1][0] = mma(wf[1][ks], bb[gq & 1][k][0], acc[1][0]);
        acc[0][1] = mma(wf[0][ks], bb[gq & 1][k][1], acc[0][1]); acc[1][1] = mma(wf[1][ks], bb[gq & 1][k][1], acc[1][1]);
      }
      __builtin_amdgcn_sched_barrier(0);
    }
    drain();
#pragma unroll
    for (int i = 0; i < 4; ++i) *(u32x4*)(smem + ((s + 1) & 1) * 32768 + (i * 512 + t) * 16) = pf[i];
#pragma unroll
    for (int tk = 0; tk < 2; ++tk)
      st16(rs, oOff + (u32)s * (32 * 8192) + tk * 4096,
           u32x4{pk2(acc[0][tk][0], acc[0][tk][1]), pk2(acc[0][tk][2], acc[0][tk][3]), pk2(acc[1][tk][0], acc[1][tk][1]), pk2(acc[1][tk][2], acc[1][tk][3])});
    if (s <= 2 || s == 63) drain();
    bar();
    if (t == 0) st4(rs, FL_QK + h * FS, (u32)((s <= 2 || s == 63) ? s : s - 1));
  }
}

DI void role_VU(const Args& A, char* smem, rsrc_t rs, int h) {
  const int t = threadIdx.x, l = t & 63, w = __builtin_amdgcn_readfirstlane(t >> 6);
  const int g = l >> 4;
  int lim = SPIN_MAX;
  u32x4 wv[16], wo[4][4];
  {
    u32x4 tmp[16];
    slab_load<16, 16>(A.Wv + h * 128, 4096, t, tmp);
    slab_store<16, 16>(smem, t, tmp);
    __syncthreads();
    slab_load<64, 16>(A.Wo + (size_t)h * 128 * 512, 512, t, tmp);
#pragma unroll
    for (int ks = 0; ks < 16; ++ks) wv[ks] = frag_tr<false>(smem, 272, ks * 32, (16 * w + 4 * (l & 3)) * 2, l);
    __syncthreads();
    slab_store<64, 16>(smem, t, tmp);
    __syncthreads();
#pragma unroll
    for (int ct = 0; ct < 4; ++ct)
#pragma unroll
      for (int ks = 0; ks < 4; ++ks) wo[ct][ks] = frag_tr<true>(smem, 1040, ks * 32, (64 * w + 16 * ct + 4 * (l & 3)) * 2, l);
    __syncthreads();
  }
  if (w == 0) wait_flags(rs, FL_XN, 8, 0, l, lim);
  __syncthreads();
  u32x4 pf[4];
#pragma unroll
  for (int i = 0; i < 4; ++i) pf[i] = ld16(rs, OFF_XN + (i * 512 + t) * 16);
#pragma unroll
  for (int i = 0; i < 4; ++i) *(u32x4*)(smem + (i * 512 + t) * 16) = pf[i];
  bar();
  char* VL = smem + 32768;
  const int slot = (g * 16 + ((l & 15) ^ (4 * g))) * 16;
  const u32 uOff = OFF_U + (u32)(8 * w + ((l >> 3) & 1)) * 16384 + (u32)h * 512 + g * 128 + (l & 7) * 16;
  for (int s = 0; s < 64; ++s) {
    if (s == 6) { if (w == 0) wait_flags(rs, FL_XN, 64, 0, l, lim); bar(); }
#pragma unroll
    for (int i = 0; i < 4; ++i) pf[i] = ld16(rs, OFF_XN + (u32)min(s + 1, 63) * 32768 + (i * 512 + t) * 16);
    __builtin_amdgcn_sched_barrier(0);
    const char* xb = smem + slot;
    f32x4 av[2] = {};
    u32x4 bb[2][4][2];
#pragma unroll
    for (int k = 0; k < 4; ++k) { bb[0][k][0] = *(const u32x4*)(xb + k * 1024); bb[0][k][1] = *(const u32x4*)(xb + 16384 + k * 1024); }
#pragma unroll
    for (int gq = 0; gq < 4; ++gq) {
      if (gq < 3) {
#pragma unroll
        for (int k = 0; k < 4; ++k) {
          bb[(gq + 1) & 1][k][0] = *(const u32x4*)(xb + ((gq + 1) * 4 + k) * 1024);
          bb[(gq + 1) & 1][k][1] = *(const u32x4*)(xb + 16384 + ((gq + 1) * 4 + k) * 1024);
        }
      }
      __builtin_amdgcn_sched_barrier(0);
#pragma unroll
      for (int k = 0; k < 4; ++k) { av[0] = mma(wv[gq * 4 + k], bb[gq & 1][k][0], av[0]); av[1] = mma(wv[gq * 4 + k], bb[gq & 1][k][1], av[1]); }
      __builtin_amdgcn_sched_barrier(0);
    }
#pragma unroll
    for (int tk = 0; tk < 2; ++tk)
      *(u32x2*)(VL + (tk * 4 + (w >> 1)) * 1024 + l * 16 + (w & 1) * 8) = u32x2{pk2(av[tk][0], av[tk][1]), pk2(av[tk][2], av[tk][3])};
    bar();
    u32x4 vf[2][4];
#pragma unroll
    for (int tk = 0; tk < 2; ++tk)
#pragma unroll
      for (int ks = 0; ks < 4; ++ks) vf[tk][ks] = *(const u32x4*)(VL + (tk * 4 + ks) * 1024 + l * 16);
    drain();
#pragma unroll
    for (int i = 0; i < 4; ++i) *(u32x4*)(smem + (i * 512 + t) * 16) = pf[i];
#pragma unroll
    for (int ct = 0; ct < 4; ++ct) {
      f32x4 a0 = {0, 0, 0, 0}, a1 = {0, 0, 0, 0};
#pragma unroll
      for (int ks = 0; ks < 4; ++ks) { a0 = mma(vf[0][ks], wo[ct][ks], a0); a1 = mma(vf[1][ks], wo[ct][ks], a1); }
      st16(rs, uOff + (u32)s * (64 * 16384) + ct * 2 * 16384, u32x4{pk2(a0[0], a0[1]), pk2(a0[2], a0[3]), pk2(a1[0], a1[1]), pk2(a1[2], a1[3])});
    }
    if (s <= 2 || s == 63) drain();
    bar();
    if (t == 0) st4(rs, FL_VU + h * FS, (u32)((s <= 2 || s == 63) ? s : s - 1));
  }
}

DI f32x4 reduce8(char* smem, const f32x4 (&acc)[4][2], int w, int l) {
#pragma unroll
  for (int ct = 0; ct < 4; ++ct)
#pragma unroll
    for (int tk = 0; tk < 2; ++tk) *(f32x4*)(smem + ((w * 8 + ct * 2 + tk) * 64 + l) * 16) = acc[ct][tk];
  bar();
  f32x4 p[8];
#pragma unroll
  for (int i = 0; i < 8; ++i) p[i] = *(const f32x4*)(smem + ((i * 8 + w) * 64 + l) * 16);
  return ((p[0] + p[1]) + (p[2] + p[3])) + ((p[4] + p[5]) + (p[6] + p[7]));
}

DI void role_T2(const Args& A, char* smem, rsrc_t rs, int gi, int j) {
  const int t = threadIdx.x, l = t & 63, w = __builtin_amdgcn_readfirstlane(t >> 6);
  const int g = l >> 4, r = l & 15;
  int lim = SPIN_MAX, vudone = -1;
  const u32 uOff = OFF_U + (u32)(8 * j + (r >> 2)) * 16384 + (u32)(4 * w) * 512 + g * 128 + (r & 3) * 16;
  const u32 yOff = OFF_YP + (u32)((4 * w + g) * 512 + 64 * j + 8 * (r >> 2) + (r & 3)) * 16;
  const int ct = w >> 1, tk = w & 1;
  const int ecol = 64 * j + 32 * (ct >> 1) + 8 * g + 4 * (ct & 1);
  for (int s = gi; s < 64; s += 4) {
    if (vudone < s) { if (w == 0) wait_prog(rs, FL_VU, 32, s, l, vudone, lim); bar(); vudone = s; if (w == 0) vudone = s; }
    u32x4 uf[4][4];
#pragma unroll
    for (int hh = 0; hh < 4; ++hh)
#pragma unroll
      for (int c = 0; c < 4; ++c) uf[hh][c] = ld16(rs, uOff + (u32)s * (64 * 16384) + (c >> 1) * 4 * 16384 + (c & 1) * 64 + hh * 512);
    const int tok = 32 * s + 16 * tk + r;
    u32x2 xv = *(const u32x2*)(A.x + (size_t)tok * 512 + ecol);
    if (w == 0) { wait_flags_bk(rs, FL_XP, 32, s, l, lim); if (s >= 62) wait_flags(rs, FL_YE + 8 * j * FS, 8, 0, l, lim); }
    bar();
    u32x4 pf[4][2], pm[2], yf[4];
#pragma unroll
    for (int hh = 0; hh < 4; ++hh)
#pragma unroll
      for (int k = 0; k < 2; ++k) pf[hh][k] = ld16(rs, OFF_PT + (u32)((s * 32 + 4 * w + hh) * 2 + k) * 1024 + l * 16);
#pragma unroll
    for (int k = 0; k < 2; ++k) pm[k] = ld16(rs, OFF_PM + (u32)((s * 8 + w) * 2 + k) * 1024 + l * 16);
#pragma unroll
    for (int c = 0; c < 4; ++c) yf[c] = ld16(rs, yOff + (u32)s * (32 * 512 * 16) + ((c >> 1) * 32 + (c & 1) * 4) * 16);
    f32x4 acc[4][2] = {};
#pragma unroll
    for (int hh = 0; hh < 4; ++hh)
#pragma unroll
      for (int c = 0; c < 4; ++c) { acc[c][0] = mma(uf[hh][c], pf[hh][0], acc[c][0]); acc[c][1] = mma(uf[hh][c], pf[hh][1], acc[c][1]); }
#pragma unroll
    for (int c = 0; c < 4; ++c) { acc[c][0] = mma(yf[c], pm[0], acc[c][0]); acc[c][1] = mma(yf[c], pm[1], acc[c][1]); }
    f32x4 o = reduce8(smem, acc, w, l);
    u32 a0 = pk2(o[0], o[1]), a1 = pk2(o[2], o[3]);
    u32x2 hv = {pk2(lo16(xv.x) + lo16(a0), hi16(xv.x) + hi16(a0)), pk2(lo16(xv.y) + lo16(a1), hi16(xv.y) + hi16(a1))};
    st8(rs, OFF_H + (u32)(ecol >> 5) * KBS + (u32)tok * 64 + (ecol & 31) * 2, hv);
    drain();
    bar();
    if (t == 0) st4(rs, FL_T2 + (gi * 8 + j) * FS, (u32)s);
  }
}

template <int MODE>
DI void role_B128(const Args& A, char* smem, rsrc_t rs, int gi, int j) {
  const int t = threadIdx.x, l = t & 63, w = __builtin_amdgcn_readfirstlane(t >> 6);
  const int g = l >> 4, r = l & 15, cg = w & 3, tk = w >> 2;
  int lim = SPIN_MAX;
  u32x4 wf[2][16];
  {
    u32x4 tmp[16];
    const u16* W = (MODE == 0 ? A.w1 : MODE == 1 ? A.w2 : A.w3) + 128 * j;
    slab_load<16, 16>(W, MODE == 2 ? 1792 : 512, t, tmp);
    slab_store<16, 16>(smem, t, tmp);
    __syncthreads();
#pragma unroll
    for (int ks = 0; ks < 16; ++ks)
#pragma unroll
      for (int te = 0; te < 2; ++te) wf[te][ks] = frag_tr<false>(smem, 272, ks * 32, (32 * cg + 8 * (l & 3) + 4 * te) * 2, l);
    __syncthreads();
  }
  const int lg = l & 3, ltk = (l >> 2) & 3, lkq = l >> 4, ltok = 4 * w + ltk;
  const int rslot = (MODE == 1) ? l * 16 : (g * 16 + (r ^ (4 * g))) * 16;
  for (int s = gi; s < 64; s += 2) {
    u32x4 yv;
    if (MODE == 1) {
      if (w == 0) {
        wait_flags(rs, FL_B1 + (s & 1) * 4 * FS, 4, s, l, lim, SLPV);
        wait_flags(rs, FL_B1 + ((s + 1) & 1) * 4 * FS, 4, s < 63 ? s + 1 : s - 1, l, lim, SLPV);
      }
      bar();
      u32x4 v[4];
#pragma unroll
      for (int i = 0; i < 4; ++i) {
        const int f = 4 * w + i, ks = f & 15, grp = ks >> 2;
        const int sh = (grp == 0) ? -1 : (grp == 1) ? 1 : (grp == 2) ? -2 : 2;
        const int row = 32 * s + 16 * (f >> 4) + r + sh;
        const bool okr = row >= 0 && row < 2048;
        u32x4 z = ld16(rs, OFF_H1 + (u32)ks * KBS + (u32)(okr ? row : 0) * 64 + g * 16);
        v[i] = okr ? z : u32x4{0, 0, 0, 0};
      }
      yv = ld16(rs, OFF_YB + (u32)(4 * j + cg) * KBS + (u32)(32 * s + 16 * tk + r) * 64 + g * 16);
#pragma unroll
      for (int i = 0; i < 4; ++i) *(u32x4*)(smem + (4 * w + i) * 1024 + l * 16) = v[i];
    } else {
      if (w == 0) {
        if (MODE == 0) wait_flags(rs, FL_T2 + (s & 3) * 8 * FS, 8, s, l, lim, SLPV);
        else wait_flags(rs, FL_B2A + gi * 4 * FS, 4, s, l, lim, SLPV);
      }
      bar();
      u32x4 v[4];
#pragma unroll
      for (int i = 0; i < 4; ++i) v[i] = ld16(rs, (MODE == 0 ? OFF_H : OFF_X1) + (u32)(4 * i + lkq) * KBS + (u32)(32 * s + ltok) * 64 + lg * 16);
      f32x2 c[16]; float var;
      ln_center(v, c, var);
      float r1 = __builtin_amdgcn_rsqf(var + 1e-5f), rz = r1;
      if (MODE == 0) {
        rz = r1 * __builtin_amdgcn_rsqf(var * r1 * r1 + 1e-5f);
#pragma unroll
        for (int i = 0; i < 4; ++i)
          if (i == j) st16(rs, OFF_YB + (u32)(4 * i + lkq) * KBS + (u32)(32 * s + ltok) * 64 + lg * 16, ln_pack(c, i, r1));
      }
#pragma unroll
      for (int i = 0; i < 4; ++i)
        *(u32x4*)(smem + (((ltok >> 4) * 16 + 4 * i + lkq) * 64 + lg * 16 + ((ltok & 15) ^ (4 * lg))) * 16) = ln_pack(c, i, rz);
    }
    bar();
    f32x4 a0 = {0, 0, 0, 0}, a1 = {0, 0, 0, 0};
    const char* xb = smem + tk * 16384 + rslot;
#pragma unroll
    for (int ks = 0; ks < 16; ++ks) {
      u32x4 b = *(const u32x4*)(xb + ks * 1024);
      a0 = mma(wf[0][ks], b, a0); a1 = mma(wf[1][ks], b, a1);
    }
    u32x4 o;
    if (MODE == 1) {
      u32 p0 = pk2(a0[0], a0[1]), p1 = pk2(a0[2], a0[3]), p2 = pk2(a1[0], a1[1]), p3 = pk2(a1[2], a1[3]);
      o = u32x4{pk2(lo16(yv.x) + lo16(p0), hi16(yv.x) + hi16(p0)), pk2(lo16(yv.y) + lo16(p1), hi16(yv.y) + hi16(p1)),
                pk2(lo16(yv.z) + lo16(p2), hi16(yv.z) + hi16(p2)), pk2(lo16(yv.w) + lo16(p3), hi16(yv.w) + hi16(p3))};
    } else {
      o = u32x4{pk2(gelu_t(a0[0]), gelu_t(a0[1])), pk2(gelu_t(a0[2]), gelu_t(a0[3])), pk2(gelu_t(a1[0]), gelu_t(a1[1])), pk2(gelu_t(a1[2]), gelu_t(a1[3]))};
    }
    st16(rs, (MODE == 0 ? OFF_H1 : MODE == 1 ? OFF_X1 : OFF_F) + (u32)(4 * j + cg) * KBS + (u32)(32 * s + 16 * tk + r) * 64 + g * 16, o);
    drain();
    bar();
    if (t == 0) st4(rs, (MODE == 0 ? FL_B1 + gi * 4 * FS : MODE == 1 ? FL_B2A + gi * 4 * FS : FL_B2B + gi * 16 * FS) + j * FS, (u32)s);
  }
}

DI void role_B2c(const Args& A, char* smem, rsrc_t rs, int gi, int j) {
  const int t = threadIdx.x, l = t & 63, w = __builtin_amdgcn_readfirstlane(t >> 6);
  const int g = l >> 4, r = l & 15;
  int lim = SPIN_MAX;
  u32x4 wf[4][7];
#pragma unroll
  for (int rnd = 0; rnd < 2; ++rnd) {
    u32x4 tmp[14];
    slab_load<8, 14>(A.w4 + (size_t)rnd * 896 * 512 + 64 * j, 512, t, tmp);
    slab_store<8, 14>(smem, t, tmp);
    __syncthreads();
    if ((w >> 2) == rnd) {
#pragma unroll
      for (int ks = 0; ks < 7; ++ks)
#pragma unroll
        for (int c = 0; c < 4; ++c) wf[c][ks] = frag_tr<false>(smem, 144, (w & 3) * 224 + ks * 32, (32 * (c >> 1) + 8 * (l & 3) + 4 * (c & 1)) * 2, l);
    }
    __syncthreads();
  }
  const int ct = w >> 1, tk = w & 1;
  const int ecol = 64 * j + 32 * (ct >> 1) + 8 * g + 4 * (ct & 1);
  for (int s = gi; s < 64; s += 2) {
    if (w == 0) wait_flags(rs, FL_B2B + gi * 16 * FS, 14, s, l, lim, SLPV);
    bar();
    u32x4 ff[2][7];
#pragma unroll
    for (int k = 0; k < 2; ++k)
#pragma unroll
      for (int ks = 0; ks < 7; ++ks) ff[k][ks] = ld16(rs, OFF_F + (u32)(7 * w + ks) * KBS + (u32)(32 * s + 16 * k + r) * 64 + g * 16);
    const int tok = 32 * s + 16 * tk + r;
    const u32 eo = (u32)(ecol >> 5) * KBS + (u32)tok * 64 + (ecol & 31) * 2;
    u32x2 hv = ld8(rs, OFF_H + eo), xv = ld8(rs, OFF_X1 + eo);
    f32x4 acc[4][2] = {};
#pragma unroll
    for (int ks = 0; ks < 7; ++ks)
#pragma unroll
      for (int c = 0; c < 4; ++c) { acc[c][0] = mma(wf[c][ks], ff[0][ks], acc[c][0]); acc[c][1] = mma(wf[c][ks], ff[1][ks], acc[c][1]); }
    f32x4 o = reduce8(smem, acc, w, l);
    u32 a0 = pk2(o[0], o[1]), a1 = pk2(o[2], o[3]);
    u32 b0 = pk2(lo16(xv.x) + lo16(a0), hi16(xv.x) + hi16(a0)), b1 = pk2(lo16(xv.y) + lo16(a1), hi16(xv.y) + hi16(a1));
    u32x2 ov = {pk2(lo16(hv.x) + lo16(b0), hi16(hv.x) + hi16(b0)), pk2(lo16(hv.y) + lo16(b1), hi16(hv.y) + hi16(b1))};
    *(u32x2*)(A.out + (size_t)tok * 512 + ecol) = ov;
    bar();
  }
}

DI void calib(rsrc_t rs, char* ws) {
  if (threadIdx.x == 0) {
    u32 res0 = 0xFFFFFFFFu, res1 = 0xFFFFFFFFu;
    for (int rep = 0; rep < 10; ++rep) {
      u32* q = (u32*)(ws + OFF_COMM + (rep & 1) * 8192); u32 v = 0xFFFFFFFFu;
      asm volatile("global_store_dword %0, %1, off sc1\n\ts_waitcnt vmcnt(0)" :: "v"(q), "v"(v) : "memory");
      unsigned long long c1 = __builtin_amdgcn_s_memtime(); u32 r;
      asm volatile("global_load_dword %0, %1, off sc1\n\ts_waitcnt vmcnt(0)" : "=v"(r) : "v"(q) : "memory");
      unsigned long long c2 = __builtin_amdgcn_s_memtime();
      u32 d = (u32)(c2 - c1) + (r & 0);
      if (rep & 1) res1 = min(res1, d); else res0 = min(res0, d);
    }
    st4(rs, FL_SEL, res0 < res1 ? 0u : 1u);
  }
}

__global__ __launch_bounds__(512, 2) void k_mega(Args A) {
  extern __shared__ __attribute__((aligned(16))) char smem[];
  const int b = blockIdx.x, xcd = b & 7, id = (b >> 3) * 4 + (xcd & 3);
  rsrc_t rs = __builtin_amdgcn_make_buffer_rsrc((void*)A.ws, 0, WS_USED, 0x00020000);
  if (xcd < 4) {
    if (id < 32) role_X(A, smem, rs, id, xcd);
    else if (id < 96) role_Y(A, smem, rs, id - 32, xcd);
    else {
      const int a = id - 96;
      if (a == 8) calib(rs, A.ws);
      ln_task(A, rs, a);
      if (MODEV & 64) return;
      if (a < 8) role_B128<0>(A, smem, rs, a >> 2, a & 3);
      else if (a < 16) role_B128<1>(A, smem, rs, (a - 8) >> 2, a & 3);
      else role_B2c(A, smem, rs, (a - 16) >> 3, a & 7);
    }
  } else {
    if (id < 32) role_QK(A, smem, rs, id);
    else if (id < 64) role_VU(A, smem, rs, id - 32);
    else {
      fold_task(A, smem, rs, id);
      if (MODEV & 64) return;
      if (id < 96) role_T2(A, smem, rs, (id - 64) >> 3, id & 7);
      else if (id < 124) role_B128<2>(A, smem, rs, (id - 96) / 14, (id - 96) % 14);
    }
  }
}

extern "C" void kernel_launch(void* const* d_in, const int* in_sizes, int n_in, void* d_out, int out_size, void* d_ws, size_t ws_size, hipStream_t stream) {
  Args A;
  A.x = (const u16*)d_in[0]; A.mem0 = (const u16*)d_in[1];
  A.Wq = (const u16*)d_in[2]; A.Wk = (const u16*)d_in[3]; A.Wv = (const u16*)d_in[4]; A.Wo = (const u16*)d_in[5];
  A.w1 = (const u16*)d_in[8]; A.w2 = (const u16*)d_in[9]; A.w3 = (const u16*)d_in[10]; A.w4 = (const u16*)d_in[11];
  A.out = (u16*)d_out;
  char* ws = (char*)(((uintptr_t)d_ws + 2 * MiB - 1) & ~(uintptr_t)(2 * MiB - 1));
  A.ws = ws;
  hipMemsetAsync(ws, 0xFF, CTRL_BYTES, stream);
  hipFuncSetAttribute((const void*)k_mega, hipFuncAttributeMaxDynamicSharedMemorySize, LDS_BYTES);
  k_mega<<<256, 512, LDS_BYTES, stream>>>(A);
}
```

```cpp
#include <hip/hip_runtime.h>
#include <stdint.h>

typedef unsigned short u16;
typedef unsigned int u32;
typedef u32 u32x2 __attribute__((ext_vector_type(2)));
typedef u32 u32x4 __attribute__((ext_vector_type(4)));
typedef u32 u32x8 __attribute__((ext_vector_type(8)));
typedef float f32x2 __attribute__((ext_vector_type(2)));
typedef float f32x4 __attribute__((ext_vector_type(4)));
typedef short s16x4 __attribute__((ext_vector_type(4)));
typedef __bf16 bf16x2 __attribute__((ext_vector_type(2)));
typedef __bf16 bf16x8 __attribute__((ext_vector_type(8)));
typedef __bf16 bf16x16 __attribute__((ext_vector_type(16)));
typedef __attribute__((address_space(3))) s16x4* lds_s16x4_p;
#define DI __device__ __forceinline__

constexpr u32 MiB = 1u << 20;
constexpr u32 OFF_COMM = 0;
constexpr u32 OFF_FLAG = 768 * 1024;
constexpr u32 OFF_KX = 2 * MiB;
constexpr u32 OFF_QX = 18 * MiB;
constexpr u32 OFF_U  = 34 * MiB;
constexpr u32 OFF_BF = 98 * MiB;
constexpr u32 OFF_YP = 114 * MiB;
constexpr u32 OFF_XN = 130 * MiB;
constexpr u32 OFF_PT = 132 * MiB;
constexpr u32 OFF_PM = 136 * MiB;
constexpr u32 OFF_H  = 137 * MiB;
constexpr u32 OFF_YB = 139 * MiB;
constexpr u32 OFF_H1 = 141 * MiB;
constexpr u32 OFF_X1 = 143 * MiB;
constexpr u32 OFF_F  = 145 * MiB;
constexpr u32 WS_USED = 152 * MiB;
constexpr u32 KBS = 2048 * 64;
constexpr u32 FS = 128;
constexpr u32 FL_SEL = OFF_FLAG, FL_XN = FL_SEL + FS, FL_FOLD = FL_XN + 64 * FS, FL_QK = FL_FOLD + 128 * FS, FL_VU = FL_QK + 32 * FS,
              FL_XP = FL_VU + 32 * FS, FL_YE = FL_XP + 32 * FS, FL_T2 = FL_YE + 64 * FS, FL_B1 = FL_T2 + 32 * FS, FL_B2A = FL_B1 + 8 * FS,
              FL_B2B = FL_B2A + 8 * FS, FL_END = FL_B2B + 32 * FS;
constexpr u32 CTRL_BYTES = FL_END;
constexpr int SC1 = 16;
constexpr int LDS_MAIN = 144 * 1024;
constexpr int LDS_BYTES = LDS_MAIN;

DI u32 pk2(float a, float b) { f32x2 f = {a, b}; return __builtin_bit_cast(u32, __builtin_convertvector(f, bf16x2)); }
DI float lo16(u32 u) { return __uint_as_float(u << 16); }
DI float hi16(u32 u) { return __uint_as_float(u & 0xFFFF0000u); }
DI void bar() { asm volatile("" ::: "memory"); __builtin_amdgcn_s_waitcnt(0xC07F); __builtin_amdgcn_s_barrier(); asm volatile("" ::: "memory"); }
DI void drain() { asm volatile("" ::: "memory"); __builtin_amdgcn_s_waitcnt(0x0F70); asm volatile("" ::: "memory"); }
DI u32 lo8(u32 x) { return __builtin_amdgcn_update_dpp(x, x, 0x128, 0xf, 0xC, false); }
DI u32 hi8(u32 x) { return __builtin_amdgcn_update_dpp(x, x, 0x128, 0xf, 0x3, false); }
DI u32 now32() { return (u32)__builtin_amdgcn_s_memtime(); }
DI void wait_for(u32 t0, int d) { while ((int)(now32() - t0) < d) __builtin_amdgcn_s_sleep(1); }
DI float max6_red(float a, float b, float c, float d, float e, float f) {
  float t, u;
  asm("v_max3_f32 %0, %2, %3, %4\n\tv_max3_f32 %1, %5, %6, %7\n\tv_max_f32 %0, %0, %1\n\ts_nop 1\n\t"
      "v_max_f32_dpp %0, %0, %0 row_ror:8 row_mask:0xf bank_mask:0xf\n\tv_mov_b32 %1, %0\n\ts_nop 1\n\t"
      "v_permlane16_swap_b32 %0, %1\n\tv_max_f32 %0, %0, %1\n\tv_mov_b32 %1, %0\n\ts_nop 1\n\t"
      "v_permlane32_swap_b32 %0, %1\n\tv_max_f32 %0, %0, %1"
      : "=&v"(t), "=&v"(u) : "v"(a), "v"(b), "v"(c), "v"(d), "v"(e), "v"(f));
  return t;
}
DI float max3_red(float a, float b, float c) {
  float t, u;
  asm("v_max3_f32 %0, %2, %3, %4\n\ts_nop 1\n\t"
      "v_max_f32_dpp %0, %0, %0 row_ror:8 row_mask:0xf bank_mask:0xf\n\tv_mov_b32 %1, %0\n\ts_nop 1\n\t"
      "v_permlane16_swap_b32 %0, %1\n\tv_max_f32 %0, %0, %1\n\tv_mov_b32 %1, %0\n\ts_nop 1\n\t"
      "v_permlane32_swap_b32 %0, %1\n\tv_max_f32 %0, %0, %1"
      : "=&v"(t), "=&v"(u) : "v"(a), "v"(b), "v"(c));
  return t;
}
#define SB() __builtin_amdgcn_sched_barrier(0)
DI void hold(u32 t0, int ticks) { while ((int)((u32)__builtin_amdgcn_s_memrealtime() - t0) < ticks) __builtin_amdgcn_s_sleep(32); }
DI float xor8f(float x) { return __uint_as_float(__builtin_amdgcn_update_dpp(0u, __float_as_uint(x), 0x128, 0xf, 0xf, false)); }
DI float xor1f(float x) { return __uint_as_float(__builtin_amdgcn_update_dpp(0u, __float_as_uint(x), 0xB1, 0xf, 0xf, false)); }
DI float xor2f(float x) { return __uint_as_float(__builtin_amdgcn_update_dpp(0u, __float_as_uint(x), 0x4E, 0xf, 0xf, false)); }
DI float max1632(float x) {
  auto s = __builtin_amdgcn_permlane16_swap(__float_as_uint(x), __float_as_uint(x), false, false);
  x = fmaxf(__uint_as_float(s[0]), __uint_as_float(s[1]));
  auto t = __builtin_amdgcn_permlane32_swap(__float_as_uint(x), __float_as_uint(x), false, false);
  return fmaxf(__uint_as_float(t[0]), __uint_as_float(t[1]));
}
DI float sum1632(float x) {
  auto s = __builtin_amdgcn_permlane16_swap(__float_as_uint(x), __float_as_uint(x), false, false);
  x = __uint_as_float(s[0]) + __uint_as_float(s[1]);
  auto t = __builtin_amdgcn_permlane32_swap(__float_as_uint(x), __float_as_uint(x), false, false);
  return __uint_as_float(t[0]) + __uint_as_float(t[1]);
}
DI float red_max4(float x) { return max1632(fmaxf(x, xor8f(x))); }
DI float red_sum4(float x) { return sum1632(x + xor8f(x)); }
DI float red_sumq(float x) { x += xor1f(x); x += xor2f(x); return sum1632(x); }
DI f32x4 smf(u32x4 a, u32x8 b, f32x4 c, int idx) {
  return __builtin_amdgcn_smfmac_f32_16x16x64_bf16(__builtin_bit_cast(bf16x8, a), __builtin_bit_cast(bf16x16, b), c, idx, 0, 0);
}
DI f32x4 mma(u32x4 a, u32x4 b, f32x4 c) {
  return __builtin_amdgcn_mfma_f32_16x16x32_bf16(__builtin_bit_cast(bf16x8, a), __builtin_bit_cast(bf16x8, b), c, 0, 0, 0);
}
DI float gelu_t(float x) {
  float u = x * fmaf(x * x, 0.044715f * 0.7978845608028654f, 0.7978845608028654f);
  float e = __builtin_amdgcn_exp2f(u * (-2.f * 1.4426950408889634f));
  return x * __builtin_amdgcn_rcpf(1.f + e);
}
typedef __amdgpu_buffer_rsrc_t rsrc_t;
DI u32x4 ld16(rsrc_t r, u32 off) { return __builtin_amdgcn_raw_buffer_load_b128(r, off, 0, SC1); }
DI u32x2 ld8(rsrc_t r, u32 off) { return __builtin_amdgcn_raw_buffer_load_b64(r, off, 0, SC1); }
DI u32 ld4(rsrc_t r, u32 off) { return __builtin_amdgcn_raw_buffer_load_b32(r, off, 0, SC1); }
DI void st16(rsrc_t r, u32 off, u32x4 v) { __builtin_amdgcn_raw_buffer_store_b128(v, r, off, 0, SC1); }
DI void st8(rsrc_t r, u32 off, u32x2 v) { __builtin_amdgcn_raw_buffer_store_b64(v, r, off, 0, SC1); }
DI void st4(rsrc_t r, u32 off, u32 v) { __builtin_amdgcn_raw_buffer_store_b32(v, r, off, 0, SC1); }
constexpr int SPIN_MAX = 1 << 18;

struct Args {
  const u16* x; const u16* mem0; const u16* Wq; const u16* Wk; const u16* Wv; const u16* Wo;
  const u16* w1; const u16* w2; const u16* w3; const u16* w4; u16* out; char* ws;
};

DI void wait_until(unsigned long long t0, int d) {
  while ((long long)(__builtin_amdgcn_s_memtime() - t0) < (long long)d) __builtin_amdgcn_s_sleep(1);
}
DI void wait_flags(rsrc_t rs, u32 off, int n, int need, int l, int& lim, int slp = 0) {
  for (int it = 0; it < lim; ++it) {
    int v = (int)ld4(rs, off + (l < n ? l : 0) * FS);
    if (__all(v >= need)) return;
    __builtin_amdgcn_s_sleep(2);
    for (int z = 0; z < slp; ++z) __builtin_amdgcn_s_sleep(16);
  }
  lim = 1;
}
DI int wave_min(int v) {
  v = min(v, (int)__builtin_amdgcn_update_dpp(0u, (u32)v, 0xB1, 0xf, 0xf, false));
  v = min(v, (int)__builtin_amdgcn_update_dpp(0u, (u32)v, 0x4E, 0xf, 0xf, false));
  v = min(v, (int)__builtin_amdgcn_update_dpp(0u, (u32)v, 0x141, 0xf, 0xf, false));
  v = min(v, (int)__builtin_amdgcn_update_dpp(0u, (u32)v, 0x140, 0xf, 0xf, false));
  auto s = __builtin_amdgcn_permlane16_swap((u32)v, (u32)v, false, false);
  v = min((int)s[0], (int)s[1]);
  auto t = __builtin_amdgcn_permlane32_swap((u32)v, (u32)v, false, false);
  v = min((int)t[0], (int)t[1]);
  return __builtin_amdgcn_readfirstlane(v);
}
DI void wait_flags_bk(rsrc_t rs, u32 off, int n, int need, int l, int& lim) {
  for (int it = 0; it < lim; ++it) {
    int v = (int)ld4(rs, off + (l < n ? l : 0) * FS);
    if (__all(v >= need)) return;
    int d = need - wave_min(v);
    __builtin_amdgcn_s_sleep(4);
    for (int z = 1; z < min(d, 8); ++z) __builtin_amdgcn_s_sleep(48);
  }
  lim = 1;
}
DI void wait_prog(rsrc_t rs, u32 off, int n, int need, int l, int& cache, int& lim) {
  if (cache >= need) return;
  for (int it = 0; it < lim; ++it) {
    int v = (int)ld4(rs, off + (l < n ? l : 0) * FS);
    if (__all(v >= need)) { cache = wave_min(v); return; }
    __builtin_amdgcn_s_sleep(2);
  }
  lim = 1;
}
DI u32x4 poll_mem(rsrc_t rs, u32 off, int& lim) {
  u32x4 m = {0xFFFFFFFFu, 0, 0, 0};
  bool ok = false;
  int it = 0;
  for (; it < lim; ++it) {
    if (!ok) m = ld16(rs, off);
    u32 mx = max(max(m.x, m.y), max(m.z, m.w));
    ok = mx != 0xFFFFFFFFu;
    if (__all(ok)) break;
    asm volatile("" ::: "memory");
  }
  if (it >= lim) lim = 1;
  return m;
}

DI bool mem_ok(const u32x4& m) { return __all(max(max(m.x, m.y), max(m.z, m.w)) != 0xFFFFFFFFu); }
#define MEMF() asm volatile("" ::: "memory")
template <int GAP>
DI void poll_mem3(rsrc_t rs, u32 off, u32 ldsAddr, int& lim, u32x4& pa, u32x4& pb, u32x4& pc, int& itc) {
  u32 t; int it;
#define PLD(R) "buffer_load_dwordx4 " R ", %[off], %[rs], 0 offen sc1\n\t"
#define PCK(x, y, z, w_, L) "s_waitcnt vmcnt(2)\n\tv_max_u32 %[t], " z ", " w_ "\n\tv_max3_u32 %[t], " x ", " y ", %[t]\n\tv_cmp_ne_u32 vcc, -1, %[t]\n\ts_cmp_eq_u64 vcc, exec\n\ts_cbranch_scc1 " L "\n\t"
  asm volatile(
      PLD("v[240:243]") "s_sleep %[gap]\n\t" PLD("v[244:247]") "s_sleep %[gap]\n\t" PLD("v[248:251]")
      "s_mov_b32 %[it], 0\n"
      "1:\n\t"
      PCK("v240", "v241", "v242", "v243", "2f") PLD("v[240:243]")
      PCK("v244", "v245", "v246", "v247", "3f") PLD("v[244:247]")
      PCK("v248", "v249", "v250", "v251", "4f") PLD("v[248:251]")
      "s_add_i32 %[it], %[it], 1\n\ts_cmp_lt_i32 %[it], %[lim]\n\ts_cbranch_scc1 1b\n\t"
      "s_waitcnt vmcnt(0)\n"
      "4:\n\tds_write_b128 %[lds], v[248:251]\n\ts_branch 5f\n"
      "3:\n\tds_write_b128 %[lds], v[244:247]\n\ts_branch 5f\n"
      "2:\n\tds_write_b128 %[lds], v[240:243]\n"
      "5:"
      : "={v[240:243]}"(pa), "={v[244:247]}"(pb), "={v[248:251]}"(pc), [t] "=&v"(t), [it] "=&s"(it)
      : [off] "v"(off), [rs] "s"(rs), [lim] "s"(lim), [lds] "v"(ldsAddr), [gap] "n"(GAP)
      : "vcc", "scc", "memory");
#undef PLD
#undef PCK
  if (it >= lim) lim = 1;
  itc = it;
}
template <int N>
DI void poll_retire(u32x4& pa, u32x4& pb, u32x4& pc) {
  asm volatile("s_waitcnt vmcnt(%3)" :: "{v[240:243]}"(pa), "{v[244:247]}"(pb), "{v[248:251]}"(pc), "n"(N));
}

template <bool KPERM>
DI u32x4 frag_tr(const char* L, int stride, int row0, int cb, int l) {
  const int g = l >> 4, q = (l & 15) >> 2;
  const int ra = row0 + (KPERM ? 4 * g : 8 * g) + q, rb = ra + (KPERM ? 16 : 4);
  u32x2 a = __builtin_bit_cast(u32x2, __builtin_amdgcn_ds_read_tr16_b64_v4i16((lds_s16x4_p)(L + ra * stride + cb)));
  u32x2 b = __builtin_bit_cast(u32x2, __builtin_amdgcn_ds_read_tr16_b64_v4i16((lds_s16x4_p)(L + rb * stride + cb)));
  return u32x4{a.x, a.y, b.x, b.y};
}
template <int PPR, int NP>
DI void slab_load(const u16* W, int ld, int t, u32x4 (&tmp)[NP]) {
#pragma unroll
  for (int i = 0; i < NP; ++i) { int id = i * 512 + t; tmp[i] = *(const u32x4*)(W + (size_t)(id / PPR) * ld + (id % PPR) * 8); }
}
template <int PPR, int NP>
DI void slab_store(char* L, int t, const u32x4 (&tmp)[NP]) {
#pragma unroll
  for (int i = 0; i < NP; ++i) { int id = i * 512 + t; *(u32x4*)(L + (id / PPR) * (PPR * 16 + 16) + (id % PPR) * 16) = tmp[i]; }
}

DI void ln_center(const u32x4 (&v)[4], f32x2 (&c)[16], float& var) {
  f32x2 s2 = {0.f, 0.f};
#pragma unroll
  for (int i = 0; i < 4; ++i)
#pragma unroll
    for (int k = 0; k < 4; ++k) { c[i * 4 + k] = f32x2{lo16(v[i][k]), hi16(v[i][k])}; s2 += c[i * 4 + k]; }
  float mean = red_sumq(s2.x + s2.y) * (1.f / 512.f);
  f32x2 m2 = {mean, mean}, q2 = {0.f, 0.f};
#pragma unroll
  for (int i = 0; i < 16; ++i) { c[i] -= m2; q2 += c[i] * c[i]; }
  var = red_sumq(q2.x + q2.y) * (1.f / 512.f);
}
DI u32x4 ln_pack(const f32x2 (&c)[16], int i, float r) {
  f32x2 r2 = {r, r};
  u32x4 o;
#pragma unroll
  for (int k = 0; k < 4; ++k) { f32x2 z = c[i * 4 + k] * r2; o[k] = pk2(z.x, z.y); }
  return o;
}

template <class F>
DI void project(const u32x8 (&wf)[2][8], const char* memL, int l, int idx, float (&v)[4], F&& extra) {
  f32x4 a0 = {0, 0, 0, 0}, a1 = {0, 0, 0, 0};
  u32x4 fr[8];
#pragma unroll
  for (int i = 0; i < 8; ++i) fr[i] = *(const u32x4*)(memL + (i << 10) + l * 16);
  __builtin_amdgcn_sched_barrier(0);
  extra();
  __builtin_amdgcn_sched_barrier(0);
#pragma unroll
  for (int i = 0; i < 8; ++i) { a0 = smf(fr[i], wf[0][i], a0, idx); a1 = smf(fr[i], wf[1][i], a1, idx); }
#pragma unroll
  for (int r = 0; r < 4; ++r) {
    auto s = __builtin_amdgcn_permlane32_swap(__float_as_uint(a0[r]), __float_as_uint(a1[r]), false, false);
    v[r] = __uint_as_float(s[0]) + __uint_as_float(s[1]);
  }
}

#define XGAP 3
#define SLPV 0
#define BD0 1000
#define BD1 1200
#define MODEV 0
#define XD 0
#define DYV 1800
#define DYMV 800
#define YREP 4
#define PRM(j) (j)
#define PRMP(j) (j)
#define PLAY 0
#define ADEC 16
#define AINC 128

DI void role_X(const Args& A, char* smem, rsrc_t rs, int h, int rep, u32 t0) {
  const int t = threadIdx.x, l = t & 63, w = __builtin_amdgcn_readfirstlane(t >> 6);
  const int g = l >> 4;
  int lim = SPIN_MAX;
  u32x8 wf[2][8];
  hold(t0, XD);
  {
    char* st = smem + w * (128 * 80);
    const u16* W = (w < 4 ? A.Wq : A.Wk) + h * 128 + (w & 3) * 32;
#pragma unroll
    for (int c = 0; c < 4; ++c) {
      u32x4 tmp[8];
#pragma unroll
      for (int i = 0; i < 8; ++i) {
        int row = (l >> 2) + 16 * i;
        int kc = 2 * c + (row >> 6);
        tmp[i] = *(const u32x4*)(W + (size_t)(kc * 64 + (row & 63)) * 4096 + (l & 3) * 8);
      }
#pragma unroll
      for (int i = 0; i < 8; ++i) *(u32x4*)(st + ((l >> 2) + 16 * i) * 80 + (l & 3) * 16) = tmp[i];
      asm volatile("s_waitcnt lgkmcnt(0)" ::: "memory");
      const int q = (l & 15) >> 2, p = l & 3;
      const int rbase = 16 * (g >> 1) + 4 * (g & 1) + (q & 1) + 8 * (q >> 1);
#pragma unroll
      for (int sl = 0; sl < 2; ++sl)
#pragma unroll
        for (int tt = 0; tt < 2; ++tt)
#pragma unroll
          for (int u = 0; u < 4; ++u) {
            int row = sl * 64 + rbase + 32 * (u >> 1) + 2 * (u & 1);
            s16x4 r = __builtin_amdgcn_ds_read_tr16_b64_v4i16((lds_s16x4_p)(st + row * 80 + (tt * 16 + p * 4) * 2));
            u32x2 r2 = __builtin_bit_cast(u32x2, r);
            wf[tt][2 * c + sl][2 * u] = r2.x; wf[tt][2 * c + sl][2 * u + 1] = r2.y;
          }
      asm volatile("s_waitcnt lgkmcnt(0)" ::: "memory");
    }
  }
  wait_flags(rs, FL_SEL, 1, 0, l, lim);
  u32 sel = __builtin_amdgcn_readfirstlane(ld4(rs, FL_SEL)) & 1;
  if (MODEV & 1) sel = 0;
  if (MODEV & 2) sel = 1;
  __syncthreads();
  char* memL = smem;
  char* qkL = smem + 8192;
  const int idx = (l & 8) ? 0xEEEE : 0x4444;
  const u32 commB = OFF_COMM + sel * 8192;
  const u32 memOff = commB + PRM(rep) * 16384 + w * 1024 + l * 16;
  const u32 rst = PLAY ? 16384 : 65536;
  const u32 pOff = commB + (32 + (h >> 3) * (PLAY ? 4 : 1)) * 16384;
  const int a = (l >> 3) & 1, r8 = l & 7;
  const u32 pTok = pOff + (h & 7) * 512 + (g * 8 + r8) * 16 + a * rst;
  const u32 pMM = pOff + 4096 + (h & 7) * 128 + r8 * 16 + ((g & 1) * 2 + a) * rst;
  const float csc = 0.08838834764831845f * 1.4426950408889634f;
  const bool isT1 = (w == 1 || w == 2);
  const int tt1 = w - 1;
  char* KQL = smem + 16384;
  const u32 kqOff = (w < 6 ? OFF_KX : OFF_QX) + (u32)h * 8192 + ((w & 1) * 16 + (l & 15)) * 256 + g * 16;
  int qkdone = -1;
  u32x4 aux[8];
#define kq aux
#pragma unroll
  for (int i = 0; i < 8; ++i) aux[i] = u32x4{0, 0, 0, 0};
  if (w >= 4) {
    wait_prog(rs, FL_QK + h * FS, 1, 0, l, qkdone, lim);
#pragma unroll
    for (int i = 0; i < 4; ++i) kq[i] = ld16(rs, kqOff + i * 64);
  }
  const u32 ptOff = OFF_PT + (u32)(h * 2 + (tt1 & 1)) * 1024 + l * 16;
  const u32 pmOff = OFF_PM + (u32)((h >> 2) * 2 + (tt1 & 1)) * 1024 + (h & 3) * 256 + (l & 15) * 16 + (g & 1) * 8;
  if (w >= 4) {
#pragma unroll
    for (int i = 0; i < 4; ++i) *(u32x4*)(KQL + ((w - 4) * 4 + i) * 1024 + l * 16) = kq[i];
  }
  bar();
  if (w == 0) {
#pragma unroll
    for (int tt = 0; tt < 2; ++tt)
#pragma unroll
      for (int ks = 0; ks < 4; ++ks) aux[tt * 4 + ks] = *(const u32x4*)(KQL + (tt * 4 + ks) * 1024 + l * 16);
  }
  if (w >= 4) wait_prog(rs, FL_QK + h * FS, 1, 1, l, qkdone, lim);
  u32x4 m = *(const u32x4*)(A.mem0 + (l & 7) * 512 + w * 64 + (l >> 3) * 8);
  u32x4 pa = m, pb = m, pc = m;
  int itc = 0;
  *(u32x4*)(memL + w * 1024 + l * 16) = m;
  const u32 memLds = (u32)(size_t)(__attribute__((address_space(3))) char*)(memL + w * 1024 + l * 16);
  for (int s = 0; s < 64; ++s) {
    char* kqb = KQL + (s & 1) * 16384;
    bar();
    float v[4];
    project(wf, memL, l, idx, v, [&]() {
      if (w >= 4 && s < 63 && !(MODEV & 32)) {
#pragma unroll
        for (int i = 0; i < 4; ++i) kq[i] = ld16(rs, kqOff + (u32)(s + 1) * (32 * 8192) + i * 64);
      }
    });
    {
      *(u32x2*)(qkL + (w * 32 + (g >> 1) * 16 + (l & 15)) * 16 + (g & 1) * 8) = u32x2{pk2(v[0], v[1]), pk2(v[2], v[3])};
    }
    if (isT1) drain();
    bar();
    if (w >= 4 && s < 63 && !(MODEV & 32)) poll_retire<4>(pa, pb, pc); else poll_retire<0>(pa, pb, pc);
    if (w == 3 && s >= 1 && l == 0) st4(rs, FL_XP + h * FS, (u32)(s - 1));
    if (w == 0 && s < 63) {
      u32x4 qf[4], mf[4];
#pragma unroll
      for (int ks = 0; ks < 4; ++ks) { qf[ks] = frag_tr<false>(qkL, 16, ks * 32, (l & 1) * 8, l); SB(); }
#pragma unroll
      for (int ks = 0; ks < 4; ++ks) { mf[ks] = frag_tr<false>(qkL + 2048, 16, ks * 32, (l & 1) * 8, l); SB(); }
      float ci = g >= 2 ? -1e30f : 0.f;
      f32x4 d0 = {ci, ci, ci, ci}, d1 = {0, 0, 0, 0}, d2 = {0, 0, 0, 0};
#pragma unroll
      for (int ks = 0; ks < 4; ++ks) { d1 = mma(aux[ks], qf[ks], d1); d2 = mma(aux[4 + ks], qf[ks], d2); SB(); }
      d0 = mma(mf[0], qf[0], d0); d0 = mma(mf[1], qf[1], d0); SB();
      float e0 = a ? d2[0] : d1[0], e1 = a ? d2[1] : d1[1], e2 = a ? d2[2] : d1[2], e3 = a ? d2[3] : d1[3];
      float m4 = fmaxf(fmaxf(e0, e1), fmaxf(e2, e3));
      SB();
      d0 = mma(mf[2], qf[2], d0); d0 = mma(mf[3], qf[3], d0); SB();
      float e4 = a ? d0[2] : d0[0], e5 = a ? d0[3] : d0[1];
      float mx = max3_red(m4, e4, e5);
      float mc = -mx * csc;
      e0 = __builtin_amdgcn_exp2f(fmaf(e0, csc, mc)); e1 = __builtin_amdgcn_exp2f(fmaf(e1, csc, mc));
      e2 = __builtin_amdgcn_exp2f(fmaf(e2, csc, mc)); e3 = __builtin_amdgcn_exp2f(fmaf(e3, csc, mc));
      e4 = __builtin_amdgcn_exp2f(fmaf(e4, csc, mc)); e5 = __builtin_amdgcn_exp2f(fmaf(e5, csc, mc));
      float sum = ((e0 + e1) + (e2 + e3)) + (e4 + e5);
      sum = red_sum4(sum);
      float rl = __builtin_amdgcn_rcpf(sum);
      rl = (s & 1) ? -rl : rl;
      u32x2 pt = {pk2(e0 * rl, e1 * rl), pk2(e2 * rl, e3 * rl)};
      u32 pm = pk2(e4 * rl, e5 * rl);
      const u32x4 gt = {lo8(pt.x), lo8(pt.y), hi8(pt.x), hi8(pt.y)};
      st16(rs, pTok, gt); st16(rs, pTok + 2 * rst, gt);
      const u32 ml = lo8(pm), mh = hi8(pm);
      auto sl = __builtin_amdgcn_permlane16_swap(ml, ml, false, false);
      auto sh = __builtin_amdgcn_permlane16_swap(mh, mh, false, false);
      if (l < 32) st16(rs, pMM, u32x4{sl[0], sh[0], sl[1], sh[1]});
    }
    if (isT1 && !(MODEV & 8)) {
      __builtin_amdgcn_s_sleep(3);
      u32x4 mf[4], kt[2][4], qt[4];
#pragma unroll
      for (int ks = 0; ks < 4; ++ks) {
        qt[ks] = *(const u32x4*)(kqb + (8 + tt1 * 4 + ks) * 1024 + l * 16);
        kt[0][ks] = *(const u32x4*)(kqb + ks * 1024 + l * 16); kt[1][ks] = *(const u32x4*)(kqb + (4 + ks) * 1024 + l * 16);
        mf[ks] = frag_tr<false>(qkL + 2048, 16, ks * 32, (l & 1) * 8, l);
      }
      f32x4 d0 = {0, 0, 0, 0}, d1 = {0, 0, 0, 0}, d2 = {0, 0, 0, 0};
#pragma unroll
      for (int ks = 0; ks < 4; ++ks) { d1 = mma(kt[0][ks], qt[ks], d1); d2 = mma(kt[1][ks], qt[ks], d2); }
#pragma unroll
      for (int ks = 0; ks < 4; ++ks) d0 = mma(mf[ks], qt[ks], d0);
      const int qi = tt1 * 16 + (l & 15);
      float e[12];
#pragma unroll
      for (int r = 0; r < 4; ++r) {
        e[r] = (4 * g + r <= qi) ? d1[r] : -1e30f;
        e[4 + r] = (16 + 4 * g + r <= qi) ? d2[r] : -1e30f;
        e[8 + r] = (g < 2) ? d0[r] : -1e30f;
      }
      float mx = e[0];
#pragma unroll
      for (int r = 1; r < 12; ++r) mx = fmaxf(mx, e[r]);
      mx = max1632(mx);
      float mc = -mx * csc, sum = 0.f;
#pragma unroll
      for (int r = 0; r < 12; ++r) { e[r] = __builtin_amdgcn_exp2f(fmaf(e[r], csc, mc)); sum += e[r]; }
      sum = sum1632(sum);
      float rl = __builtin_amdgcn_rcpf(sum);
      st16(rs, ptOff + (u32)s * 65536, u32x4{pk2(e[0] * rl, e[1] * rl), pk2(e[2] * rl, e[3] * rl), pk2(e[4] * rl, e[5] * rl), pk2(e[6] * rl, e[7] * rl)});
      if (g < 2) st8(rs, pmOff + (u32)s * 16384, u32x2{pk2(e[8] * rl, e[9] * rl), pk2(e[10] * rl, e[11] * rl)});
    }
    if (s < 63) {
      char* kqn = KQL + ((s + 1) & 1) * 16384;
      if (w >= 4) {
        __builtin_amdgcn_s_sleep(4);
        wait_prog(rs, FL_QK + h * FS, 1, min(s + 2, 63), l, qkdone, lim);
#pragma unroll
        for (int i = 0; i < 4; ++i) *(u32x4*)(kqn + ((w - 4) * 4 + i) * 1024 + l * 16) = kq[i];
      }
      bar();
      if (w == 0) {
#pragma unroll
        for (int tt = 0; tt < 2; ++tt)
#pragma unroll
          for (int ks = 0; ks < 4; ++ks) aux[tt * 4 + ks] = *(const u32x4*)(kqn + (tt * 4 + ks) * 1024 + l * 16);
      }
      poll_mem3<XGAP>(rs, memOff + ((s + 1) & 3) * 131072, memLds, lim, pa, pb, pc, itc);
    }
  }
  drain();
  bar();
  if (t == 192) st4(rs, FL_XP + h * FS, 63u);
}

#undef kq
DI void fold_task(const Args& A, char* smem, rsrc_t rs, int task) {
  const int t = threadIdx.x, l = t & 63, w = __builtin_amdgcn_readfirstlane(t >> 6);
  const int g = l >> 4, h = task >> 2, q = task & 3;
  const int r = l & 15, gr = r >> 2, rg = r & 3;
  const int drow = 64 * w + 16 * (gr >> 1) + 4 * (gr & 1) + 8 * (rg >> 1) + (rg & 1);
  const u16* Wv = A.Wv + (size_t)drow * 4096 + h * 128 + g * 8;
  u32x4 tmp[4], af[4][4];
  slab_load<16, 4>(A.Wo + (size_t)h * 128 * 512 + 128 * q, 512, t, tmp);
#pragma unroll
  for (int u = 0; u < 4; ++u)
#pragma unroll
    for (int ks = 0; ks < 4; ++ks) af[u][ks] = *(const u32x4*)(Wv + (size_t)(32 * (u >> 1) + 2 * (u & 1)) * 4096 + ks * 32);
  __builtin_amdgcn_sched_barrier(0);
  slab_store<16, 4>(smem, t, tmp);
  __syncthreads();
#pragma unroll
  for (int ct = 0; ct < 8; ++ct) {
    u32x4 wo[4];
#pragma unroll
    for (int ks = 0; ks < 4; ++ks) wo[ks] = frag_tr<false>(smem, 272, ks * 32, (16 * ct + 4 * (l & 3)) * 2, l);
    const int c = 128 * q + 16 * ct + r, cc = c >> 3, jj = c & 7;
#pragma unroll
    for (int half = 0; half < 2; ++half) {
      f32x4 a0 = {0, 0, 0, 0}, a1 = {0, 0, 0, 0};
#pragma unroll
      for (int ks = 0; ks < 4; ++ks) { a0 = mma(af[2 * half][ks], wo[ks], a0); a1 = mma(af[2 * half + 1][ks], wo[ks], a1); }
      const u32 o = (u32)((((((cc * 8 + (h >> 2)) * 2 + ((h >> 1) & 1)) * 8 + w) * 2 + half) * 64 + g * 16 + (h & 1) * 8 + jj)) * 16;
      st16(rs, OFF_BF + o, u32x4{pk2(a0[0], a0[1]), pk2(a0[2], a0[3]), pk2(a1[0], a1[1]), pk2(a1[2], a1[3])});
    }
  }
  drain();
  __syncthreads();
  if (t == 0) st4(rs, FL_FOLD + task * FS, 1u);
}

DI void ln_task(const Args& A, rsrc_t rs, int s0) {
  const int t = threadIdx.x, l = t & 63, w = __builtin_amdgcn_readfirstlane(t >> 6);
  const int g = l & 3, tk = (l >> 2) & 3, kq = l >> 4, tok = 4 * w + tk;
  u32x4 v[2][4];
#pragma unroll
  for (int u = 0; u < 2; ++u)
#pragma unroll
    for (int i = 0; i < 4; ++i) v[u][i] = *(const u32x4*)(A.x + (size_t)(32 * (s0 + 32 * u) + tok) * 512 + 32 * (4 * i + kq) + 8 * g);
#pragma unroll
  for (int u = 0; u < 2; ++u) {
    f32x2 c[16]; float var;
    ln_center(v[u], c, var);
    float r = __builtin_amdgcn_rsqf(var + 1e-5f);
#pragma unroll
    for (int i = 0; i < 4; ++i)
      st16(rs, OFF_XN + (u32)(s0 + 32 * u) * 32768 + (u32)((((tok >> 4) * 16 + 4 * i + kq) * 64) + g * 16 + ((tok & 15) ^ (4 * g))) * 16, ln_pack(c, i, r));
  }
  drain();
  __syncthreads();
  if (t < 2) st4(rs, FL_XN + (s0 + 32 * t) * FS, 1u);
}

DI void role_Y(const Args& A, char* smem, rsrc_t rs, int cc, int rep) {
  const int t = threadIdx.x, l = t & 63, w = __builtin_amdgcn_readfirstlane(t >> 6);
  const int g = l >> 4;
  int lim = SPIN_MAX;
  fold_task(A, smem, rs, (((cc & 3) + 4 * (cc >> 4)) << 2) | ((cc >> 2) & 3));
  for (int it = 0; it < lim; ++it) {
    int v = (int)ld4(rs, FL_FOLD + (u32)((4 * w + (l & 3)) * 4 + (cc >> 4)) * FS);
    if (__all(v >= 0)) break;
    __builtin_amdgcn_s_sleep(2);
  }
  u32x8 wf[2][8];
#pragma unroll
  for (int tt = 0; tt < 2; ++tt)
#pragma unroll
    for (int i = 0; i < 8; ++i) {
      int kc = i;
      u32 o = OFF_BF + (u32)((((cc * 8 + w) * 2 + tt) * 8 + kc) * 2) * 1024 + l * 16;
      u32x4 lo = ld16(rs, o), hi = ld16(rs, o + 1024);
      wf[tt][i] = u32x8{lo.x, lo.y, lo.z, lo.w, hi.x, hi.y, hi.z, hi.w};
    }
  wait_flags(rs, FL_SEL, 1, 0, l, lim);
  u32 sel = __builtin_amdgcn_readfirstlane(ld4(rs, FL_SEL)) & 1;
  if (MODEV & 1) sel = 0;
  if (MODEV & 2) sel = 1;
  char* memL = smem;
  char* YL = smem + 8192;
  float* RL = (float*)(smem + 12288);
  const int idx = (l & 8) ? 0xEEEE : 0x4444;
  const u32 commB = OFF_COMM + sel * 8192;
  const u32 memOff = commB + (PRM(rep) + 4) * 16384 + w * 1024 + l * 16;
  const int set = (l >> 3) & 1, r8 = l & 7;
  const u32 pBase = commB + (32 + (PLAY ? (w & 3) * 4 + PRMP(rep) : rep * 4 + (w & 3))) * 16384;
  const u32 pTok = pBase + set * 512 + (g * 8 + r8) * 16;
  const u32 pMM = pBase + 4096 + (set * 4 + g) * 128 + r8 * 16;
  const u32 uOff = OFF_U + (u32)cc * 16384 + (u32)(8 * (w & 3) + set) * 512 + g * 128 + r8 * 16;
  char* UL = smem + 16384;
  int vudone = -1;
  u32x4 un[4];
  if (w >= 4) {
    wait_prog(rs, FL_VU, 32, 0, l, vudone, lim);
#pragma unroll
    for (int mm = 0; mm < 4; ++mm) un[mm] = ld16(rs, uOff + mm * 1024);
  }
  u32x4 m = *(const u32x4*)(A.mem0 + (l & 7) * 512 + w * 64 + (l >> 3) * 8);
  const int rlIdx = (l & 7) * 8 + ((l >> 4) & 1) + 2 * (l >> 5) + 4 * ((l >> 3) & 1);
  const u32 pubOff = commB + (l >> 3) * 16384 + cc * 128 + (l & 7) * 16;
  u32 tref = now32();
  int dP = DYV;
  for (int s = 0; s < 64; ++s) {
    *(u32x4*)(memL + w * 1024 + l * 16) = m;
    char* ub = UL + (s & 1) * 16384 + (w & 3) * 4096 + l * 16;
    if (w >= 4) {
#pragma unroll
      for (int mm = 0; mm < 4; ++mm) *(u32x4*)(ub + mm * 1024) = un[mm];
    }
    bar();
    if (w >= 4 && s < 62 && !(MODEV & 16)) {
      wait_prog(rs, FL_VU, 32, s + 1, l, vudone, lim);
#pragma unroll
      for (int mm = 0; mm < 4; ++mm) un[mm] = ld16(rs, uOff + (u32)(s + 1) * (64 * 16384) + mm * 1024);
    }
    if (w == 5 && s >= 2)
      st16(rs, commB + (((s - 1) & 3) * 8 + (l >> 3)) * 16384 + cc * 128 + (l & 7) * 16, u32x4{0xFFFFFFFFu, 0xFFFFFFFFu, 0xFFFFFFFFu, 0xFFFFFFFFu});
    float v[4];
    u32x4 uf[4];
    project(wf, memL, l, idx, v, [&]() {
      if (w < 4) {
#pragma unroll
        for (int mm = 0; mm < 4; ++mm) uf[mm] = *(const u32x4*)(ub + mm * 1024);
      }
    });
    {
      u32x2 pk = {pk2(v[0], v[1]), pk2(v[2], v[3])};
      int head = 4 * w + 2 * (g >> 1) + ((l >> 3) & 1);
      *(u32x2*)(YL + (head * 8 + (l & 7)) * 16 + (g & 1) * 8) = pk;
    }
    bar();
    if (w >= 5 && s < 61) wait_prog(rs, FL_VU, 32, s + 2, l, vudone, lim);
    if (w == 4 && !(MODEV & 128)) {
#pragma unroll
      for (int i = 0; i < 4; ++i) {
        int id = i * 64 + l;
        u32x4 y = *(const u32x4*)(YL + id * 16);
        st16(rs, OFF_YP + (u32)((s * 32 + (id >> 3)) * 512 + cc * 8 + (id & 7)) * 16, y);
      }
    }
    if (w == 4 && s < 61) wait_prog(rs, FL_VU, 32, s + 2, l, vudone, lim);
    if (s < 63) {
      if (w < 4) {
        u32x4 yf = *(const u32x4*)(YL + ((8 * w + 4 * set + g) * 8 + r8) * 16);
        u32x4 pf[5];
        wait_for(tref, dP);
        const u32 tm = (s & 1) ? 0x80008000u : 0u;
        bool ok0 = false, ok1 = false, ok2 = false, ok3 = false, ok4 = false;
        int it = 0;
        for (; it < lim; ++it) {
          if (!ok0) pf[0] = ld16(rs, pTok);
          if (!ok1) pf[1] = ld16(rs, pTok + 1024);
          if (!ok2) pf[2] = ld16(rs, pTok + 2048);
          if (!ok3) pf[3] = ld16(rs, pTok + 3072);
          if (!ok4) pf[4] = ld16(rs, pMM);
          ok0 = (((pf[0].x ^ tm) | (pf[0].z ^ tm)) & 0x80008000u) == 0;
          ok1 = (((pf[1].x ^ tm) | (pf[1].z ^ tm)) & 0x80008000u) == 0;
          ok2 = (((pf[2].x ^ tm) | (pf[2].z ^ tm)) & 0x80008000u) == 0;
          ok3 = (((pf[3].x ^ tm) | (pf[3].z ^ tm)) & 0x80008000u) == 0;
          ok4 = (((pf[4].x ^ tm) | (pf[4].y ^ tm) | (pf[4].z ^ tm) | (pf[4].w ^ tm)) & 0x80008000u) == 0;
          if (__all(ok0 && ok1 && ok2 && ok3 && ok4)) break;
          asm volatile("" ::: "memory");
        }
        if (it >= lim) lim = 1;
        dP += (it == 0) ? -ADEC : min(it, 4) * AINC;
        dP = max(min(dP, 6000), 0);
        f32x4 d = {0, 0, 0, 0}, d2 = {0, 0, 0, 0};
        d = mma(pf[0], uf[0], d); d2 = mma(pf[1], uf[1], d2); d = mma(pf[2], uf[2], d); d2 = mma(pf[3], uf[3], d2); d = mma(pf[4], yf, d);
        d += d2;
        if ((g >> 1) == set) {
          float* dst = RL + (w * 2 + set) * 64 + (4 * (g & 1)) * 8 + r8;
          dst[0] = d[0]; dst[8] = d[1]; dst[16] = d[2]; dst[24] = d[3];
        }
      }
      if (w == 4) drain();
      bar();
      if (w == 0) {
        const float* R = RL + rlIdx;
        float x0 = R[0], x1 = R[64], x2 = R[128], x3 = R[192], x4 = R[256], x5 = R[320], x6 = R[384], x7 = R[448];
        float sum = ((x0 + x1) + (x2 + x3)) + ((x4 + x5) + (x6 + x7));
        auto s16 = __builtin_amdgcn_permlane16_swap(__float_as_uint(sum), __float_as_uint(sum), false, false);
        u32 pk = pk2(__uint_as_float(s16[0]), __uint_as_float(s16[1]));
        if (s & 1) pk ^= 0x80008000u;
        pk = min(pk, 0xFFFFFFFEu);
        auto s32 = __builtin_amdgcn_permlane32_swap(pk, pk, false, false);
        u32x4 o = {lo8(s32[0]), lo8(s32[1]), hi8(s32[0]), hi8(s32[1])};
        if (l < 32 || YREP) st16(rs, pubOff + ((s + 1) & 3) * 131072, o);
      }
      const u32 tpub = now32();
      wait_for(tpub, DYMV);
      m = poll_mem(rs, memOff + ((s + 1) & 3) * 131072, lim);
      tref = now32();
      drain();
    }
  }
  drain();
  bar();
  if (t == 0) st4(rs, FL_YE + cc * FS, 1u);
}

DI void role_QK(const Args& A, char* smem, rsrc_t rs, int h) {
  const int t = threadIdx.x, l = t & 63, w = __builtin_amdgcn_readfirstlane(t >> 6);
  const int g = l >> 4;
  int lim = SPIN_MAX;
  u32x4 wf[2][16];
  {
    u32x4 tmp[2][16];
#pragma unroll
    for (int rnd = 0; rnd < 2; ++rnd)
#pragma unroll
      for (int i = 0; i < 16; ++i) {
        int id = i * 512 + t, row = id >> 5, c8 = id & 31;
        tmp[rnd][i] = *(const u32x4*)((c8 < 16 ? A.Wq : A.Wk) + (size_t)(rnd * 256 + row) * 4096 + h * 128 + (c8 & 15) * 8);
      }
    SB();
#pragma unroll
    for (int rnd = 0; rnd < 2; ++rnd) {
      slab_store<32, 16>(smem, t, tmp[rnd]);
      __syncthreads();
#pragma unroll
      for (int ks = 0; ks < 8; ++ks)
#pragma unroll
        for (int te = 0; te < 2; ++te) wf[te][rnd * 8 + ks] = frag_tr<false>(smem, 528, ks * 32, (32 * w + 8 * (l & 3) + 4 * te) * 2, l);
      __syncthreads();
    }
  }
  if (w == 0) wait_flags(rs, FL_XN, 8, 0, l, lim);
  __syncthreads();
  u32x4 pf[4];
#pragma unroll
  for (int i = 0; i < 4; ++i) pf[i] = ld16(rs, OFF_XN + (i * 512 + t) * 16);
#pragma unroll
  for (int i = 0; i < 4; ++i) *(u32x4*)(smem + (i * 512 + t) * 16) = pf[i];
  bar();
  const int slot = (g * 16 + ((l & 15) ^ (4 * g))) * 16;
  const u32 oOff = (w < 4 ? OFF_QX : OFF_KX) + (u32)h * 8192 + (l & 15) * 256 + ((w & 3) * 32 + 8 * g) * 2;
  for (int s = 0; s < 64; ++s) {
    if (s == 6) { if (w == 0) wait_flags(rs, FL_XN, 64, 0, l, lim); bar(); }
#pragma unroll
    for (int i = 0; i < 4; ++i) pf[i] = ld16(rs, OFF_XN + (u32)min(s + 1, 63) * 32768 + (i * 512 + t) * 16);
    __builtin_amdgcn_sched_barrier(0);
    const char* xb = smem + (s & 1) * 32768 + slot;
    f32x4 acc[2][2] = {};
    u32x4 bb[2][4][2];
#pragma unroll
    for (int k = 0; k < 4; ++k) { bb[0][k][0] = *(const u32x4*)(xb + k * 1024); bb[0][k][1] = *(const u32x4*)(xb + 16384 + k * 1024); }
#pragma unroll
    for (int gq = 0; gq < 4; ++gq) {
      if (gq < 3) {
#pragma unroll
        for (int k = 0; k < 4; ++k) {
          bb[(gq + 1) & 1][k][0] = *(const u32x4*)(xb + ((gq + 1) * 4 + k) * 1024);
          bb[(gq + 1) & 1][k][1] = *(const u32x4*)(xb + 16384 + ((gq + 1) * 4 + k) * 1024);
        }
      }
      __builtin_amdgcn_sched_barrier(0);
#pragma unroll
      for (int k = 0; k < 4; ++k) {
        const int ks = gq * 4 + k;
        acc[0][0] = mma(wf[0][ks], bb[gq & 1][k][0], acc[0][0]); acc[1][0] = mma(wf[1][ks], bb[gq & 1][k][0], acc[1][0]);
        acc[0][1] = mma(wf[0][ks], bb[gq & 1][k][1], acc[0][1]); acc[1][1] = mma(wf[1][ks], bb[gq & 1][k][1], acc[1][1]);
      }
      __builtin_amdgcn_sched_barrier(0);
    }
    drain();
#pragma unroll
    for (int i = 0; i < 4; ++i) *(u32x4*)(smem + ((s + 1) & 1) * 32768 + (i * 512 + t) * 16) = pf[i];
#pragma unroll
    for (int tk = 0; tk < 2; ++tk)
      st16(rs, oOff + (u32)s * (32 * 8192) + tk * 4096,
           u32x4{pk2(acc[0][tk][0], acc[0][tk][1]), pk2(acc[0][tk][2], acc[0][tk][3]), pk2(acc[1][tk][0], acc[1][tk][1]), pk2(acc[1][tk][2], acc[1][tk][3])});
    if (s <= 2 || s == 63) drain();
    bar();
    if (t == 0) st4(rs, FL_QK + h * FS, (u32)((s <= 2 || s == 63) ? s : s - 1));
  }
}

DI void role_VU(const Args& A, char* smem, rsrc_t rs, int h) {
  const int t = threadIdx.x, l = t & 63, w = __builtin_amdgcn_readfirstlane(t >> 6);
  const int g = l >> 4;
  int lim = SPIN_MAX;
  u32x4 wv[16], wo[4][4];
  {
    u32x4 tmp[16];
    slab_load<16, 16>(A.Wv + h * 128, 4096, t, tmp);
    slab_store<16, 16>(smem, t, tmp);
    __syncthreads();
    slab_load<64, 16>(A.Wo + (size_t)h * 128 * 512, 512, t, tmp);
#pragma unroll
    for (int ks = 0; ks < 16; ++ks) wv[ks] = frag_tr<false>(smem, 272, ks * 32, (16 * w + 4 * (l & 3)) * 2, l);
    __syncthreads();
    slab_store<64, 16>(smem, t, tmp);
    __syncthreads();
#pragma unroll
    for (int ct = 0; ct < 4; ++ct)
#pragma unroll
      for (int ks = 0; ks < 4; ++ks) wo[ct][ks] = frag_tr<true>(smem, 1040, ks * 32, (64 * w + 16 * ct + 4 * (l & 3)) * 2, l);
    __syncthreads();
  }
  if (w == 0) wait_flags(rs, FL_XN, 8, 0, l, lim);
  __syncthreads();
  u32x4 pf[4];
#pragma unroll
  for (int i = 0; i < 4; ++i) pf[i] = ld16(rs, OFF_XN + (i * 512 + t) * 16);
#pragma unroll
  for (int i = 0; i < 4; ++i) *(u32x4*)(smem + (i * 512 + t) * 16) = pf[i];
  bar();
  char* VL = smem + 32768;
  const int slot = (g * 16 + ((l & 15) ^ (4 * g))) * 16;
  const u32 uOff = OFF_U + (u32)(8 * w + ((l >> 3) & 1)) * 16384 + (u32)h * 512 + g * 128 + (l & 7) * 16;
  for (int s = 0; s < 64; ++s) {
    if (s == 6) { if (w == 0) wait_flags(rs, FL_XN, 64, 0, l, lim); bar(); }
#pragma unroll
    for (int i = 0; i < 4; ++i) pf[i] = ld16(rs, OFF_XN + (u32)min(s + 1, 63) * 32768 + (i * 512 + t) * 16);
    __builtin_amdgcn_sched_barrier(0);
    const char* xb = smem + slot;
    f32x4 av[2] = {};
    u32x4 bb[2][4][2];
#pragma unroll
    for (int k = 0; k < 4; ++k) { bb[0][k][0] = *(const u32x4*)(xb + k * 1024); bb[0][k][1] = *(const u32x4*)(xb + 16384 + k * 1024); }
#pragma unroll
    for (int gq = 0; gq < 4; ++gq) {
      if (gq < 3) {
#pragma unroll
        for (int k = 0; k < 4; ++k) {
          bb[(gq + 1) & 1][k][0] = *(const u32x4*)(xb + ((gq + 1) * 4 + k) * 1024);
          bb[(gq + 1) & 1][k][1] = *(const u32x4*)(xb + 16384 + ((gq + 1) * 4 + k) * 1024);
        }
      }
      __builtin_amdgcn_sched_barrier(0);
#pragma unroll
      for (int k = 0; k < 4; ++k) { av[0] = mma(wv[gq * 4 + k], bb[gq & 1][k][0], av[0]); av[1] = mma(wv[gq * 4 + k], bb[gq & 1][k][1], av[1]); }
      __builtin_amdgcn_sched_barrier(0);
    }
#pragma unroll
    for (int tk = 0; tk < 2; ++tk)
      *(u32x2*)(VL + (tk * 4 + (w >> 1)) * 1024 + l * 16 + (w & 1) * 8) = u32x2{pk2(av[tk][0], av[tk][1]), pk2(av[tk][2], av[tk][3])};
    bar();
    u32x4 vf[2][4];
#pragma unroll
    for (int tk = 0; tk < 2; ++tk)
#pragma unroll
      for (int ks = 0; ks < 4; ++ks) vf[tk][ks] = *(const u32x4*)(VL + (tk * 4 + ks) * 1024 + l * 16);
    drain();
#pragma unroll
    for (int i = 0; i < 4; ++i) *(u32x4*)(smem + (i * 512 + t) * 16) = pf[i];
#pragma unroll
    for (int ct = 0; ct < 4; ++ct) {
      f32x4 a0 = {0, 0, 0, 0}, a1 = {0, 0, 0, 0};
#pragma unroll
      for (int ks = 0; ks < 4; ++ks) { a0 = mma(vf[0][ks], wo[ct][ks], a0); a1 = mma(vf[1][ks], wo[ct][ks], a1); }
      st16(rs, uOff + (u32)s * (64 * 16384) + ct * 2 * 16384, u32x4{pk2(a0[0], a0[1]), pk2(a0[2], a0[3]), pk2(a1[0], a1[1]), pk2(a1[2], a1[3])});
    }
    if (s <= 2 || s == 63) drain();
    bar();
    if (t == 0) st4(rs, FL_VU + h * FS, (u32)((s <= 2 || s == 63) ? s : s - 1));
  }
}

DI f32x4 reduce8(char* smem, const f32x4 (&acc)[4][2], int w, int l) {
#pragma unroll
  for (int ct = 0; ct < 4; ++ct)
#pragma unroll
    for (int tk = 0; tk < 2; ++tk) *(f32x4*)(smem + ((w * 8 + ct * 2 + tk) * 64 + l) * 16) = acc[ct][tk];
  bar();
  f32x4 p[8];
#pragma unroll
  for (int i = 0; i < 8; ++i) p[i] = *(const f32x4*)(smem + ((i * 8 + w) * 64 + l) * 16);
  return ((p[0] + p[1]) + (p[2] + p[3])) + ((p[4] + p[5]) + (p[6] + p[7]));
}

DI void role_T2(const Args& A, char* smem, rsrc_t rs, int gi, int j) {
  const int t = threadIdx.x, l = t & 63, w = __builtin_amdgcn_readfirstlane(t >> 6);
  const int g = l >> 4, r = l & 15;
  int lim = SPIN_MAX, vudone = -1;
  const u32 uOff = OFF_U + (u32)(8 * j + (r >> 2)) * 16384 + (u32)(4 * w) * 512 + g * 128 + (r & 3) * 16;
  const u32 yOff = OFF_YP + (u32)((4 * w + g) * 512 + 64 * j + 8 * (r >> 2) + (r & 3)) * 16;
  const int ct = w >> 1, tk = w & 1;
  const int ecol = 64 * j + 32 * (ct >> 1) + 8 * g + 4 * (ct & 1);
  for (int s = gi; s < 64; s += 4) {
    if (vudone < s) { if (w == 0) wait_prog(rs, FL_VU, 32, s, l, vudone, lim); bar(); vudone = s; if (w == 0) vudone = s; }
    u32x4 uf[4][4];
#pragma unroll
    for (int hh = 0; hh < 4; ++hh)
#pragma unroll
      for (int c = 0; c < 4; ++c) uf[hh][c] = ld16(rs, uOff + (u32)s * (64 * 16384) + (c >> 1) * 4 * 16384 + (c & 1) * 64 + hh * 512);
    const int tok = 32 * s + 16 * tk + r;
    u32x2 xv = *(const u32x2*)(A.x + (size_t)tok * 512 + ecol);
    if (w == 0) {
      wait_flags_bk(rs, FL_XP, 32, s, l, lim);
      if (s == 63) wait_flags(rs, FL_YE + 8 * j * FS, 8, 0, l, lim);
    }
    bar();
    u32x4 pf[4][2], pm[2], yf[4];
#pragma unroll
    for (int hh = 0; hh < 4; ++hh)
#pragma unroll
      for (int k = 0; k < 2; ++k) pf[hh][k] = ld16(rs, OFF_PT + (u32)((s * 32 + 4 * w + hh) * 2 + k) * 1024 + l * 16);
#pragma unroll
    for (int k = 0; k < 2; ++k) pm[k] = ld16(rs, OFF_PM + (u32)((s * 8 + w) * 2 + k) * 1024 + l * 16);
#pragma unroll
    for (int c = 0; c < 4; ++c) yf[c] = ld16(rs, yOff + (u32)s * (32 * 512 * 16) + ((c >> 1) * 32 + (c & 1) * 4) * 16);
    SB();
    f32x4 acc[4][2] = {};
#pragma unroll
    for (int hh = 0; hh < 4; ++hh)
#pragma unroll
      for (int c = 0; c < 4; ++c) { acc[c][0] = mma(uf[hh][c], pf[hh][0], acc[c][0]); acc[c][1] = mma(uf[hh][c], pf[hh][1], acc[c][1]); }
#pragma unroll
    for (int c = 0; c < 4; ++c) { acc[c][0] = mma(yf[c], pm[0], acc[c][0]); acc[c][1] = mma(yf[c], pm[1], acc[c][1]); }
    f32x4 o = reduce8(smem, acc, w, l);
    u32 a0 = pk2(o[0], o[1]), a1 = pk2(o[2], o[3]);
    u32x2 hv = {pk2(lo16(xv.x) + lo16(a0), hi16(xv.x) + hi16(a0)), pk2(lo16(xv.y) + lo16(a1), hi16(xv.y) + hi16(a1))};
    st8(rs, OFF_H + (u32)(ecol >> 5) * KBS + (u32)tok * 64 + (ecol & 31) * 2, hv);
    drain();
    bar();
    if (t == 0) st4(rs, FL_T2 + (gi * 8 + j) * FS, (u32)s);
  }
}

template <int MODE>
DI void role_B128(const Args& A, char* smem, rsrc_t rs, int gi, int j, u32 t0) {
  const int t = threadIdx.x, l = t & 63, w = __builtin_amdgcn_readfirstlane(t >> 6);
  const int g = l >> 4, r = l & 15, cg = w & 3, tk = w >> 2;
  int lim = SPIN_MAX;
  u32x4 wf[2][16];
  hold(t0, MODE == 0 ? BD0 : BD1);
  {
    u32x4 tmp[16];
    const u16* W = (MODE == 0 ? A.w1 : MODE == 1 ? A.w2 : A.w3) + 128 * j;
    slab_load<16, 16>(W, MODE == 2 ? 1792 : 512, t, tmp);
    slab_store<16, 16>(smem, t, tmp);
    __syncthreads();
#pragma unroll
    for (int ks = 0; ks < 16; ++ks)
#pragma unroll
      for (int te = 0; te < 2; ++te) wf[te][ks] = frag_tr<false>(smem, 272, ks * 32, (32 * cg + 8 * (l & 3) + 4 * te) * 2, l);
    __syncthreads();
  }
  const int lg = l & 3, ltk = (l >> 2) & 3, lkq = l >> 4, ltok = 4 * w + ltk;
  const int rslot = (MODE == 1) ? l * 16 : (g * 16 + (r ^ (4 * g))) * 16;
  for (int s = gi; s < 64; s += 2) {
    u32x4 yv;
    if (MODE == 1) {
      if (w == 0) {
        wait_flags(rs, FL_B1 + (s & 1) * 4 * FS, 4, s, l, lim, SLPV);
        wait_flags(rs, FL_B1 + ((s + 1) & 1) * 4 * FS, 4, s < 63 ? s + 1 : s - 1, l, lim, SLPV);
      }
      bar();
      u32x4 v[4];
#pragma unroll
      for (int i = 0; i < 4; ++i) {
        const int f = 4 * w + i, ks = f & 15, grp = ks >> 2;
        const int sh = (grp == 0) ? -1 : (grp == 1) ? 1 : (grp == 2) ? -2 : 2;
        const int row = 32 * s + 16 * (f >> 4) + r + sh;
        const bool okr = row >= 0 && row < 2048;
        u32x4 z = ld16(rs, OFF_H1 + (u32)ks * KBS + (u32)(okr ? row : 0) * 64 + g * 16);
        v[i] = okr ? z : u32x4{0, 0, 0, 0};
      }
      yv = ld16(rs, OFF_YB + (u32)(4 * j + cg) * KBS + (u32)(32 * s + 16 * tk + r) * 64 + g * 16);
      SB();
#pragma unroll
      for (int i = 0; i < 4; ++i) *(u32x4*)(smem + (4 * w + i) * 1024 + l * 16) = v[i];
    } else {
      if (w == 0) {
        if (MODE == 0) wait_flags(rs, FL_T2 + (s & 3) * 8 * FS, 8, s, l, lim, SLPV);
        else wait_flags(rs, FL_B2A + gi * 4 * FS, 4, s, l, lim, SLPV);
      }
      bar();
      u32x4 v[4];
#pragma unroll
      for (int i = 0; i < 4; ++i) v[i] = ld16(rs, (MODE == 0 ? OFF_H : OFF_X1) + (u32)(4 * i + lkq) * KBS + (u32)(32 * s + ltok) * 64 + lg * 16);
      SB();
      f32x2 c[16]; float var;
      ln_center(v, c, var);
      float r1 = __builtin_amdgcn_rsqf(var + 1e-5f), rz = r1;
      if (MODE == 0) {
        rz = r1 * __builtin_amdgcn_rsqf(var * r1 * r1 + 1e-5f);
#pragma unroll
        for (int i = 0; i < 4; ++i)
          if (i == j) st16(rs, OFF_YB + (u32)(4 * i + lkq) * KBS + (u32)(32 * s + ltok) * 64 + lg * 16, ln_pack(c, i, r1));
      }
#pragma unroll
      for (int i = 0; i < 4; ++i)
        *(u32x4*)(smem + (((ltok >> 4) * 16 + 4 * i + lkq) * 64 + lg * 16 + ((ltok & 15) ^ (4 * lg))) * 16) = ln_pack(c, i, rz);
    }
    bar();
    f32x4 a0 = {0, 0, 0, 0}, a1 = {0, 0, 0, 0};
    const char* xb = smem + tk * 16384 + rslot;
#pragma unroll
    for (int ks = 0; ks < 16; ++ks) {
      u32x4 b = *(const u32x4*)(xb + ks * 1024);
      a0 = mma(wf[0][ks], b, a0); a1 = mma(wf[1][ks], b, a1);
    }
    u32x4 o;
    if (MODE == 1) {
      u32 p0 = pk2(a0[0], a0[1]), p1 = pk2(a0[2], a0[3]), p2 = pk2(a1[0], a1[1]), p3 = pk2(a1[2], a1[3]);
      o = u32x4{pk2(lo16(yv.x) + lo16(p0), hi16(yv.x) + hi16(p0)), pk2(lo16(yv.y) + lo16(p1), hi16(yv.y) + hi16(p1)),
                pk2(lo16(yv.z) + lo16(p2), hi16(yv.z) + hi16(p2)), pk2(lo16(yv.w) + lo16(p3), hi16(yv.w) + hi16(p3))};
    } else {
      o = u32x4{pk2(gelu_t(a0[0]), gelu_t(a0[1])), pk2(gelu_t(a0[2]), gelu_t(a0[3])), pk2(gelu_t(a1[0]), gelu_t(a1[1])), pk2(gelu_t(a1[2]), gelu_t(a1[3]))};
    }
    st16(rs, (MODE == 0 ? OFF_H1 : MODE == 1 ? OFF_X1 : OFF_F) + (u32)(4 * j + cg) * KBS + (u32)(32 * s + 16 * tk + r) * 64 + g * 16, o);
    drain();
    bar();
    if (t == 0) st4(rs, (MODE == 0 ? FL_B1 + gi * 4 * FS : MODE == 1 ? FL_B2A + gi * 4 * FS : FL_B2B + gi * 16 * FS) + j * FS, (u32)s);
  }
}

DI void role_B2c(const Args& A, char* smem, rsrc_t rs, int gi, int j, u32 t0) {
  const int t = threadIdx.x, l = t & 63, w = __builtin_amdgcn_readfirstlane(t >> 6);
  const int g = l >> 4, r = l & 15;
  int lim = SPIN_MAX;
  u32x4 wf[4][7];
  hold(t0, BD1);
#pragma unroll
  for (int rnd = 0; rnd < 2; ++rnd) {
    u32x4 tmp[14];
    slab_load<8, 14>(A.w4 + (size_t)rnd * 896 * 512 + 64 * j, 512, t, tmp);
    slab_store<8, 14>(smem, t, tmp);
    __syncthreads();
    if ((w >> 2) == rnd) {
#pragma unroll
      for (int ks = 0; ks < 7; ++ks)
#pragma unroll
        for (int c = 0; c < 4; ++c) wf[c][ks] = frag_tr<false>(smem, 144, (w & 3) * 224 + ks * 32, (32 * (c >> 1) + 8 * (l & 3) + 4 * (c & 1)) * 2, l);
    }
    __syncthreads();
  }
  const int ct = w >> 1, tk = w & 1;
  const int ecol = 64 * j + 32 * (ct >> 1) + 8 * g + 4 * (ct & 1);
  for (int s = gi; s < 64; s += 2) {
    if (w == 0) wait_flags(rs, FL_B2B + gi * 16 * FS, 14, s, l, lim, SLPV);
    bar();
    u32x4 ff[2][7];
#pragma unroll
    for (int k = 0; k < 2; ++k)
#pragma unroll
      for (int ks = 0; ks < 7; ++ks) ff[k][ks] = ld16(rs, OFF_F + (u32)(7 * w + ks) * KBS + (u32)(32 * s + 16 * k + r) * 64 + g * 16);
    const int tok = 32 * s + 16 * tk + r;
    const u32 eo = (u32)(ecol >> 5) * KBS + (u32)tok * 64 + (ecol & 31) * 2;
    u32x2 hv = ld8(rs, OFF_H + eo), xv = ld8(rs, OFF_X1 + eo);
    SB();
    f32x4 acc[4][2] = {};
#pragma unroll
    for (int ks = 0; ks < 7; ++ks)
#pragma unroll
      for (int c = 0; c < 4; ++c) { acc[c][0] = mma(wf[c][ks], ff[0][ks], acc[c][0]); acc[c][1] = mma(wf[c][ks], ff[1][ks], acc[c][1]); }
    f32x4 o = reduce8(smem, acc, w, l);
    u32 a0 = pk2(o[0], o[1]), a1 = pk2(o[2], o[3]);
    u32 b0 = pk2(lo16(xv.x) + lo16(a0), hi16(xv.x) + hi16(a0)), b1 = pk2(lo16(xv.y) + lo16(a1), hi16(xv.y) + hi16(a1));
    u32x2 ov = {pk2(lo16(hv.x) + lo16(b0), hi16(hv.x) + hi16(b0)), pk2(lo16(hv.y) + lo16(b1), hi16(hv.y) + hi16(b1))};
    *(u32x2*)(A.out + (size_t)tok * 512 + ecol) = ov;
    bar();
  }
}

DI void calib(rsrc_t rs, char* ws) {
  if (threadIdx.x == 0) {
    u32 res0 = 0xFFFFFFFFu, res1 = 0xFFFFFFFFu;
    for (int rep = 0; rep < 10; ++rep) {
      u32* q = (u32*)(ws + OFF_COMM + (rep & 1) * 8192); u32 v = 0xFFFFFFFFu;
      asm volatile("global_store_dword %0, %1, off sc1\n\ts_waitcnt vmcnt(0)" :: "v"(q), "v"(v) : "memory");
      unsigned long long c1 = __builtin_amdgcn_s_memtime(); u32 r;
      asm volatile("global_load_dword %0, %1, off sc1\n\ts_waitcnt vmcnt(0)" : "=v"(r) : "v"(q) : "memory");
      unsigned long long c2 = __builtin_amdgcn_s_memtime();
      u32 d = (u32)(c2 - c1) + (r & 0);
      if (rep & 1) res1 = min(res1, d); else res0 = min(res0, d);
    }
    st4(rs, FL_SEL, res0 < res1 ? 0u : 1u);
  }
}

__global__ __launch_bounds__(512, 2) void k_mega(Args A) {
  extern __shared__ __attribute__((aligned(16))) char smem[];
  const int b = blockIdx.x, xcd = b & 7, id = (b >> 3) * 4 + (xcd & 3);
  rsrc_t rs = __builtin_amdgcn_make_buffer_rsrc((void*)A.ws, 0, WS_USED, 0x00020000);
  const u32 t0 = (u32)__builtin_amdgcn_s_memrealtime();
  if (xcd < 4) {
    if (id < 32) role_X(A, smem, rs, id, xcd, t0);
    else if (id < 96) role_Y(A, smem, rs, id - 32, xcd);
    else {
      const int a = id - 96;
      if (a == 8) calib(rs, A.ws);
      ln_task(A, rs, a);
      if (MODEV & 64) return;
      if (a < 8) role_B128<0>(A, smem, rs, a >> 2, a & 3, t0);
      else if (a < 16) role_B128<1>(A, smem, rs, (a - 8) >> 2, a & 3, t0);
      else role_B2c(A, smem, rs, (a - 16) >> 3, a & 7, t0);
    }
  } else {
    if (id < 32) role_QK(A, smem, rs, id);
    else if (id < 64) role_VU(A, smem, rs, id - 32);
    else {
      fold_task(A, smem, rs, ((16 + (id & 3) + 4 * ((id - 64) >> 4)) << 2) | ((id >> 2) & 3));
      if (MODEV & 64) return;
      if (id < 96) role_T2(A, smem, rs, (id - 64) >> 3, id & 7);
      else if (id < 124) role_B128<2>(A, smem, rs, (id - 96) / 14, (id - 96) % 14, t0);
    }
  }
}

extern "C" void kernel_launch(void* const* d_in, const int* in_sizes, int n_in, void* d_out, int out_size, void* d_ws, size_t ws_size, hipStream_t stream) {
  Args A;
  A.x = (const u16*)d_in[0]; A.mem0 = (const u16*)d_in[1];
  A.Wq = (const u16*)d_in[2]; A.Wk = (const u16*)d_in[3]; A.Wv = (const u16*)d_in[4]; A.Wo = (const u16*)d_in[5];
  A.w1 = (const u16*)d_in[8]; A.w2 = (const u16*)d_in[9]; A.w3 = (const u16*)d_in[10]; A.w4 = (const u16*)d_in[11];
  A.out = (u16*)d_out;
  char* ws = (char*)(((uintptr_t)d_ws + 2 * MiB - 1) & ~(uintptr_t)(2 * MiB - 1));
  A.ws = ws;
  hipMemsetAsync(ws, 0xFF, CTRL_BYTES, stream);
  hipFuncSetAttribute((const void*)k_mega, hipFuncAttributeMaxDynamicSharedMemorySize, LDS_BYTES);
  k_mega<<<256, 512, LDS_BYTES, stream>>>(A);
}
```

```cpp
#include <hip/hip_runtime.h>
#include <stdint.h>

typedef unsigned short u16;
typedef unsigned int u32;
typedef u32 u32x2 __attribute__((ext_vector_type(2)));
typedef u32 u32x4 __attribute__((ext_vector_type(4)));
typedef u32 u32x8 __attribute__((ext_vector_type(8)));
typedef float f32x2 __attribute__((ext_vector_type(2)));
typedef float f32x4 __attribute__((ext_vector_type(4)));
typedef short s16x4 __attribute__((ext_vector_type(4)));
typedef __bf16 bf16x2 __attribute__((ext_vector_type(2)));
typedef __bf16 bf16x8 __attribute__((ext_vector_type(8)));
typedef __bf16 bf16x16 __attribute__((ext_vector_type(16)));
typedef __attribute__((address_space(3))) s16x4* lds_s16x4_p;
#define DI __device__ __forceinline__

constexpr u32 MiB = 1u << 20;
constexpr u32 OFF_COMM = 0;
constexpr u32 OFF_FLAG = 768 * 1024;
constexpr u32 OFF_KX = 2 * MiB;
constexpr u32 OFF_QX = 18 * MiB;
constexpr u32 OFF_U  = 34 * MiB;
constexpr u32 OFF_BF = 98 * MiB;
constexpr u32 OFF_YP = 114 * MiB;
constexpr u32 OFF_XN = 130 * MiB;
constexpr u32 OFF_PT = 132 * MiB;
constexpr u32 OFF_PM = 136 * MiB;
constexpr u32 OFF_H  = 137 * MiB;
constexpr u32 OFF_YB = 139 * MiB;
constexpr u32 OFF_H1 = 141 * MiB;
constexpr u32 OFF_X1 = 143 * MiB;
constexpr u32 OFF_F  = 145 * MiB;
constexpr u32 WS_USED = 152 * MiB;
constexpr u32 KBS = 2048 * 64;
constexpr u32 FS = 128;
constexpr u32 FL_SEL = OFF_FLAG, FL_XN = FL_SEL + FS, FL_FOLD = FL_XN + 64 * FS, FL_QK = FL_FOLD + 128 * FS, FL_VU = FL_QK + 32 * FS,
              FL_XP = FL_VU + 32 * FS, FL_YE = FL_XP + 32 * FS, FL_T2 = FL_YE + 64 * FS, FL_B1 = FL_T2 + 32 * FS, FL_B2A = FL_B1 + 8 * FS,
              FL_B2B = FL_B2A + 8 * FS, FL_END = FL_B2B + 32 * FS;
constexpr u32 CTRL_BYTES = FL_END;
constexpr int SC1 = 16;
constexpr int LDS_MAIN = 144 * 1024;
constexpr int LDS_BYTES = LDS_MAIN;

DI u32 pk2(float a, float b) { f32x2 f = {a, b}; return __builtin_bit_cast(u32, __builtin_convertvector(f, bf16x2)); }
DI float lo16(u32 u) { return __uint_as_float(u << 16); }
DI float hi16(u32 u) { return __uint_as_float(u & 0xFFFF0000u); }
DI void bar() { asm volatile("" ::: "memory"); __builtin_amdgcn_s_waitcnt(0xC07F); __builtin_amdgcn_s_barrier(); asm volatile("" ::: "memory"); }
DI void drain() { asm volatile("" ::: "memory"); __builtin_amdgcn_s_waitcnt(0x0F70); asm volatile("" ::: "memory"); }
DI u32 lo8(u32 x) { return __builtin_amdgcn_update_dpp(x, x, 0x128, 0xf, 0xC, false); }
DI u32 hi8(u32 x) { return __builtin_amdgcn_update_dpp(x, x, 0x128, 0xf, 0x3, false); }
DI u32 now32() { return (u32)__builtin_amdgcn_s_memtime(); }
DI void wait_for(u32 t0, int d) { while ((int)(now32() - t0) < d) __builtin_amdgcn_s_sleep(1); }
DI float max6_red(float a, float b, float c, float d, float e, float f) {
  float t, u;
  asm("v_max3_f32 %0, %2, %3, %4\n\tv_max3_f32 %1, %5, %6, %7\n\tv_max_f32 %0, %0, %1\n\ts_nop 1\n\t"
      "v_max_f32_dpp %0, %0, %0 row_ror:8 row_mask:0xf bank_mask:0xf\n\tv_mov_b32 %1, %0\n\ts_nop 1\n\t"
      "v_permlane16_swap_b32 %0, %1\n\tv_max_f32 %0, %0, %1\n\tv_mov_b32 %1, %0\n\ts_nop 1\n\t"
      "v_permlane32_swap_b32 %0, %1\n\tv_max_f32 %0, %0, %1"
      : "=&v"(t), "=&v"(u) : "v"(a), "v"(b), "v"(c), "v"(d), "v"(e), "v"(f));
  return t;
}
DI float max3_red(float a, float b, float c) {
  float t, u;
  asm("v_max3_f32 %0, %2, %3, %4\n\ts_nop 1\n\t"
      "v_max_f32_dpp %0, %0, %0 row_ror:8 row_mask:0xf bank_mask:0xf\n\tv_mov_b32 %1, %0\n\ts_nop 1\n\t"
      "v_permlane16_swap_b32 %0, %1\n\tv_max_f32 %0, %0, %1\n\tv_mov_b32 %1, %0\n\ts_nop 1\n\t"
      "v_permlane32_swap_b32 %0, %1\n\tv_max_f32 %0, %0, %1"
      : "=&v"(t), "=&v"(u) : "v"(a), "v"(b), "v"(c));
  return t;
}
#define SB() __builtin_amdgcn_sched_barrier(0)
DI void hold(u32 t0, int ticks) { while ((int)((u32)__builtin_amdgcn_s_memrealtime() - t0) < ticks) __builtin_amdgcn_s_sleep(32); }
DI float xor8f(float x) { return __uint_as_float(__builtin_amdgcn_update_dpp(0u, __float_as_uint(x), 0x128, 0xf, 0xf, false)); }
DI float xor1f(float x) { return __uint_as_float(__builtin_amdgcn_update_dpp(0u, __float_as_uint(x), 0xB1, 0xf, 0xf, false)); }
DI float xor2f(float x) { return __uint_as_float(__builtin_amdgcn_update_dpp(0u, __float_as_uint(x), 0x4E, 0xf, 0xf, false)); }
DI float max1632(float x) {
  auto s = __builtin_amdgcn_permlane16_swap(__float_as_uint(x), __float_as_uint(x), false, false);
  x = fmaxf(__uint_as_float(s[0]), __uint_as_float(s[1]));
  auto t = __builtin_amdgcn_permlane32_swap(__float_as_uint(x), __float_as_uint(x), false, false);
  return fmaxf(__uint_as_float(t[0]), __uint_as_float(t[1]));
}
DI float sum1632(float x) {
  auto s = __builtin_amdgcn_permlane16_swap(__float_as_uint(x), __float_as_uint(x), false, false);
  x = __uint_as_float(s[0]) + __uint_as_float(s[1]);
  auto t = __builtin_amdgcn_permlane32_swap(__float_as_uint(x), __float_as_uint(x), false, false);
  return __uint_as_float(t[0]) + __uint_as_float(t[1]);
}
DI float red_max4(float x) { return max1632(fmaxf(x, xor8f(x))); }
DI float red_sum4(float x) { return sum1632(x + xor8f(x)); }
DI float red_sumq(float x) { x += xor1f(x); x += xor2f(x); return sum1632(x); }
DI f32x4 smf(u32x4 a, u32x8 b, f32x4 c, int idx) {
  return __builtin_amdgcn_smfmac_f32_16x16x64_bf16(__builtin_bit_cast(bf16x8, a), __builtin_bit_cast(bf16x16, b), c, idx, 0, 0);
}
DI f32x4 mma(u32x4 a, u32x4 b, f32x4 c) {
  return __builtin_amdgcn_mfma_f32_16x16x32_bf16(__builtin_bit_cast(bf16x8, a), __builtin_bit_cast(bf16x8, b), c, 0, 0, 0);
}
DI float gelu_t(float x) {
  float u = x * fmaf(x * x, 0.044715f * 0.7978845608028654f, 0.7978845608028654f);
  float e = __builtin_amdgcn_exp2f(u * (-2.f * 1.4426950408889634f));
  return x * __builtin_amdgcn_rcpf(1.f + e);
}
typedef __amdgpu_buffer_rsrc_t rsrc_t;
DI u32x4 ld16(rsrc_t r, u32 off) { return __builtin_amdgcn_raw_buffer_load_b128(r, off, 0, SC1); }
DI u32x2 ld8(rsrc_t r, u32 off) { return __builtin_amdgcn_raw_buffer_load_b64(r, off, 0, SC1); }
DI u32 ld4(rsrc_t r, u32 off) { return __builtin_amdgcn_raw_buffer_load_b32(r, off, 0, SC1); }
DI void st16(rsrc_t r, u32 off, u32x4 v) { __builtin_amdgcn_raw_buffer_store_b128(v, r, off, 0, SC1); }
DI void st8(rsrc_t r, u32 off, u32x2 v) { __builtin_amdgcn_raw_buffer_store_b64(v, r, off, 0, SC1); }
DI void st4(rsrc_t r, u32 off, u32 v) { __builtin_amdgcn_raw_buffer_store_b32(v, r, off, 0, SC1); }
constexpr int SPIN_MAX = 1 << 18;
constexpr u32 SENT = 0xFFFFFFFFu;
DI u32 nz(u32 v) { return min(v, 0xFFFFFFFEu); }
DI u32x4 nz4(u32x4 v) { return u32x4{nz(v.x), nz(v.y), nz(v.z), nz(v.w)}; }
DI u32 mx4(const u32x4& m) { return max(max(m.x, m.y), max(m.z, m.w)); }

struct Args {
  const u16* x; const u16* mem0; const u16* Wq; const u16* Wk; const u16* Wv; const u16* Wo;
  const u16* w1; const u16* w2; const u16* w3; const u16* w4; u16* out; char* ws;
};

DI void wait_until(unsigned long long t0, int d) {
  while ((long long)(__builtin_amdgcn_s_memtime() - t0) < (long long)d) __builtin_amdgcn_s_sleep(1);
}
DI void wait_flags(rsrc_t rs, u32 off, int n, int need, int l, int& lim, int slp = 0) {
  for (int it = 0; it < lim; ++it) {
    int v = (int)ld4(rs, off + (l < n ? l : 0) * FS);
    if (__all(v >= need)) return;
    __builtin_amdgcn_s_sleep(2);
    for (int z = 0; z < slp; ++z) __builtin_amdgcn_s_sleep(16);
  }
  lim = 1;
}
DI int wave_min(int v) {
  v = min(v, (int)__builtin_amdgcn_update_dpp(0u, (u32)v, 0xB1, 0xf, 0xf, false));
  v = min(v, (int)__builtin_amdgcn_update_dpp(0u, (u32)v, 0x4E, 0xf, 0xf, false));
  v = min(v, (int)__builtin_amdgcn_update_dpp(0u, (u32)v, 0x141, 0xf, 0xf, false));
  v = min(v, (int)__builtin_amdgcn_update_dpp(0u, (u32)v, 0x140, 0xf, 0xf, false));
  auto s = __builtin_amdgcn_permlane16_swap((u32)v, (u32)v, false, false);
  v = min((int)s[0], (int)s[1]);
  auto t = __builtin_amdgcn_permlane32_swap((u32)v, (u32)v, false, false);
  v = min((int)t[0], (int)t[1]);
  return __builtin_amdgcn_readfirstlane(v);
}
DI void wait_flags_bk(rsrc_t rs, u32 off, int n, int need, int l, int& lim) {
  for (int it = 0; it < lim; ++it) {
    int v = (int)ld4(rs, off + (l < n ? l : 0) * FS);
    if (__all(v >= need)) return;
    int d = need - wave_min(v);
    __builtin_amdgcn_s_sleep(4);
    for (int z = 1; z < min(d, 8); ++z) __builtin_amdgcn_s_sleep(48);
  }
  lim = 1;
}
DI void wait_prog(rsrc_t rs, u32 off, int n, int need, int l, int& cache, int& lim) {
  if (cache >= need) return;
  for (int it = 0; it < lim; ++it) {
    int v = (int)ld4(rs, off + (l < n ? l : 0) * FS);
    if (__all(v >= need)) { cache = wave_min(v); return; }
    __builtin_amdgcn_s_sleep(2);
  }
  lim = 1;
}
DI u32x4 poll_mem(rsrc_t rs, u32 off, int& lim) {
  u32x4 m = {0xFFFFFFFFu, 0, 0, 0};
  bool ok = false;
  int it = 0;
  for (; it < lim; ++it) {
    if (!ok) m = ld16(rs, off);
    u32 mx = max(max(m.x, m.y), max(m.z, m.w));
    ok = mx != 0xFFFFFFFFu;
    if (__all(ok)) break;
    asm volatile("" ::: "memory");
  }
  if (it >= lim) lim = 1;
  return m;
}

DI bool mem_ok(const u32x4& m) { return __all(max(max(m.x, m.y), max(m.z, m.w)) != 0xFFFFFFFFu); }
#define MEMF() asm volatile("" ::: "memory")
template <int GAP>
DI void poll_mem3(rsrc_t rs, u32 off, u32 ldsAddr, int& lim, u32x4& pa, u32x4& pb, u32x4& pc, int& itc) {
  u32 t; int it;
#define PLD(R) "buffer_load_dwordx4 " R ", %[off], %[rs], 0 offen sc1\n\t"
#define PCK(x, y, z, w_, L) "s_waitcnt vmcnt(2)\n\tv_max_u32 %[t], " z ", " w_ "\n\tv_max3_u32 %[t], " x ", " y ", %[t]\n\tv_cmp_ne_u32 vcc, -1, %[t]\n\ts_cmp_eq_u64 vcc, exec\n\ts_cbranch_scc1 " L "\n\t"
  asm volatile(
      PLD("v[240:243]") "s_sleep %[gap]\n\t" PLD("v[244:247]") "s_sleep %[gap]\n\t" PLD("v[248:251]")
      "s_mov_b32 %[it], 0\n"
      "1:\n\t"
      PCK("v240", "v241", "v242", "v243", "2f") PLD("v[240:243]")
      PCK("v244", "v245", "v246", "v247", "3f") PLD("v[244:247]")
      PCK("v248", "v249", "v250", "v251", "4f") PLD("v[248:251]")
      "s_add_i32 %[it], %[it], 1\n\ts_cmp_lt_i32 %[it], %[lim]\n\ts_cbranch_scc1 1b\n\t"
      "s_waitcnt vmcnt(0)\n"
      "4:\n\tds_write_b128 %[lds], v[248:251]\n\ts_branch 5f\n"
      "3:\n\tds_write_b128 %[lds], v[244:247]\n\ts_branch 5f\n"
      "2:\n\tds_write_b128 %[lds], v[240:243]\n"
      "5:"
      : "={v[240:243]}"(pa), "={v[244:247]}"(pb), "={v[248:251]}"(pc), [t] "=&v"(t), [it] "=&s"(it)
      : [off] "v"(off), [rs] "s"(rs), [lim] "s"(lim), [lds] "v"(ldsAddr), [gap] "n"(GAP)
      : "vcc", "scc", "memory");
#undef PLD
#undef PCK
  if (it >= lim) lim = 1;
  itc = it;
}
template <int N>
DI void poll_retire(u32x4& pa, u32x4& pb, u32x4& pc) {
  asm volatile("s_waitcnt vmcnt(%3)" :: "{v[240:243]}"(pa), "{v[244:247]}"(pb), "{v[248:251]}"(pc), "n"(N));
}

template <bool KPERM>
DI u32x4 frag_tr(const char* L, int stride, int row0, int cb, int l) {
  const int g = l >> 4, q = (l & 15) >> 2;
  const int ra = row0 + (KPERM ? 4 * g : 8 * g) + q, rb = ra + (KPERM ? 16 : 4);
  u32x2 a = __builtin_bit_cast(u32x2, __builtin_amdgcn_ds_read_tr16_b64_v4i16((lds_s16x4_p)(L + ra * stride + cb)));
  u32x2 b = __builtin_bit_cast(u32x2, __builtin_amdgcn_ds_read_tr16_b64_v4i16((lds_s16x4_p)(L + rb * stride + cb)));
  return u32x4{a.x, a.y, b.x, b.y};
}
template <int PPR, int NP>
DI void slab_load(const u16* W, int ld, int t, u32x4 (&tmp)[NP]) {
#pragma unroll
  for (int i = 0; i < NP; ++i) { int id = i * 512 + t; tmp[i] = *(const u32x4*)(W + (size_t)(id / PPR) * ld + (id % PPR) * 8); }
}
template <int PPR, int NP>
DI void slab_store(char* L, int t, const u32x4 (&tmp)[NP]) {
#pragma unroll
  for (int i = 0; i < NP; ++i) { int id = i * 512 + t; *(u32x4*)(L + (id / PPR) * (PPR * 16 + 16) + (id % PPR) * 16) = tmp[i]; }
}

DI void ln_center(const u32x4 (&v)[4], f32x2 (&c)[16], float& var) {
  f32x2 s2 = {0.f, 0.f};
#pragma unroll
  for (int i = 0; i < 4; ++i)
#pragma unroll
    for (int k = 0; k < 4; ++k) { c[i * 4 + k] = f32x2{lo16(v[i][k]), hi16(v[i][k])}; s2 += c[i * 4 + k]; }
  float mean = red_sumq(s2.x + s2.y) * (1.f / 512.f);
  f32x2 m2 = {mean, mean}, q2 = {0.f, 0.f};
#pragma unroll
  for (int i = 0; i < 16; ++i) { c[i] -= m2; q2 += c[i] * c[i]; }
  var = red_sumq(q2.x + q2.y) * (1.f / 512.f);
}
DI u32x4 ln_pack(const f32x2 (&c)[16], int i, float r) {
  f32x2 r2 = {r, r};
  u32x4 o;
#pragma unroll
  for (int k = 0; k < 4; ++k) { f32x2 z = c[i * 4 + k] * r2; o[k] = pk2(z.x, z.y); }
  return o;
}

template <class F>
DI void project(const u32x8 (&wf)[2][8], const char* memL, int l, int idx, float (&v)[4], F&& extra) {
  f32x4 a0 = {0, 0, 0, 0}, a1 = {0, 0, 0, 0};
  u32x4 fr[8];
#pragma unroll
  for (int i = 0; i < 8; ++i) fr[i] = *(const u32x4*)(memL + (i << 10) + l * 16);
  __builtin_amdgcn_sched_barrier(0);
  extra();
  __builtin_amdgcn_sched_barrier(0);
#pragma unroll
  for (int i = 0; i < 8; ++i) { a0 = smf(fr[i], wf[0][i], a0, idx); a1 = smf(fr[i], wf[1][i], a1, idx); }
#pragma unroll
  for (int r = 0; r < 4; ++r) {
    auto s = __builtin_amdgcn_permlane32_swap(__float_as_uint(a0[r]), __float_as_uint(a1[r]), false, false);
    v[r] = __uint_as_float(s[0]) + __uint_as_float(s[1]);
  }
}

#define XGAP 3
#define SLPV 0
#define BD0 1000
#define BD1 1200
#define MODEV 0
#define XD 0
#define DYV 1800
#define DYMV 800
#define YREP 4
#define PRM(j) (j)
#define PRMP(j) (j)
#define PLAY 0
#define ADEC 16
#define AINC 128

DI void role_X(const Args& A, char* smem, rsrc_t rs, int h, int rep, u32 t0) {
  const int t = threadIdx.x, l = t & 63, w = __builtin_amdgcn_readfirstlane(t >> 6);
  const int g = l >> 4;
  int lim = SPIN_MAX;
  u32x8 wf[2][8];
  hold(t0, XD);
  {
    char* st = smem + w * (128 * 80);
    const u16* W = (w < 4 ? A.Wq : A.Wk) + h * 128 + (w & 3) * 32;
#pragma unroll
    for (int c = 0; c < 4; ++c) {
      u32x4 tmp[8];
#pragma unroll
      for (int i = 0; i < 8; ++i) {
        int row = (l >> 2) + 16 * i;
        int kc = 2 * c + (row >> 6);
        tmp[i] = *(const u32x4*)(W + (size_t)(kc * 64 + (row & 63)) * 4096 + (l & 3) * 8);
      }
#pragma unroll
      for (int i = 0; i < 8; ++i) *(u32x4*)(st + ((l >> 2) + 16 * i) * 80 + (l & 3) * 16) = tmp[i];
      asm volatile("s_waitcnt lgkmcnt(0)" ::: "memory");
      const int q = (l & 15) >> 2, p = l & 3;
      const int rbase = 16 * (g >> 1) + 4 * (g & 1) + (q & 1) + 8 * (q >> 1);
#pragma unroll
      for (int sl = 0; sl < 2; ++sl)
#pragma unroll
        for (int tt = 0; tt < 2; ++tt)
#pragma unroll
          for (int u = 0; u < 4; ++u) {
            int row = sl * 64 + rbase + 32 * (u >> 1) + 2 * (u & 1);
            s16x4 r = __builtin_amdgcn_ds_read_tr16_b64_v4i16((lds_s16x4_p)(st + row * 80 + (tt * 16 + p * 4) * 2));
            u32x2 r2 = __builtin_bit_cast(u32x2, r);
            wf[tt][2 * c + sl][2 * u] = r2.x; wf[tt][2 * c + sl][2 * u + 1] = r2.y;
          }
      asm volatile("s_waitcnt lgkmcnt(0)" ::: "memory");
    }
  }
  wait_flags(rs, FL_SEL, 1, 0, l, lim);
  u32 sel = __builtin_amdgcn_readfirstlane(ld4(rs, FL_SEL)) & 1;
  if (MODEV & 1) sel = 0;
  if (MODEV & 2) sel = 1;
  __syncthreads();
  char* memL = smem;
  char* qkL = smem + 8192;
  const int idx = (l & 8) ? 0xEEEE : 0x4444;
  const u32 commB = OFF_COMM + sel * 8192;
  const u32 memOff = commB + PRM(rep) * 16384 + w * 1024 + l * 16;
  const u32 rst = PLAY ? 16384 : 65536;
  const u32 pOff = commB + (32 + (h >> 3) * (PLAY ? 4 : 1)) * 16384;
  const int a = (l >> 3) & 1, r8 = l & 7;
  const u32 pTok = pOff + (h & 7) * 512 + (g * 8 + r8) * 16 + a * rst;
  const u32 pMM = pOff + 4096 + (h & 7) * 128 + r8 * 16 + ((g & 1) * 2 + a) * rst;
  const float csc = 0.08838834764831845f * 1.4426950408889634f;
  const bool isT1 = (w == 1 || w == 2);
  const int tt1 = w - 1;
  char* KQL = smem + 16384;
  const u32 kqOff = (w < 6 ? OFF_KX : OFF_QX) + (u32)h * 8192 + ((w & 1) * 16 + (l & 15)) * 256 + g * 16;
  int qkdone = -1;
  u32x4 aux[8];
#define kq aux
#pragma unroll
  for (int i = 0; i < 8; ++i) aux[i] = u32x4{0, 0, 0, 0};
  if (w >= 4) {
    wait_prog(rs, FL_QK + h * FS, 1, 0, l, qkdone, lim);
#pragma unroll
    for (int i = 0; i < 4; ++i) kq[i] = ld16(rs, kqOff + i * 64);
  }
  const u32 ptOff = OFF_PT + (u32)(h * 2 + (tt1 & 1)) * 1024 + l * 16;
  const u32 pmOff = OFF_PM + (u32)((h >> 2) * 2 + (tt1 & 1)) * 1024 + (h & 3) * 256 + (l & 15) * 16 + (g & 1) * 8;
  if (isT1) {
    st16(rs, ptOff + 63u * 65536, u32x4{SENT, SENT, SENT, SENT});
    if (g < 2) st8(rs, pmOff + 63u * 16384, u32x2{SENT, SENT});
  }
  if (w >= 4) {
#pragma unroll
    for (int i = 0; i < 4; ++i) *(u32x4*)(KQL + ((w - 4) * 4 + i) * 1024 + l * 16) = kq[i];
  }
  bar();
  if (w == 0) {
#pragma unroll
    for (int tt = 0; tt < 2; ++tt)
#pragma unroll
      for (int ks = 0; ks < 4; ++ks) aux[tt * 4 + ks] = *(const u32x4*)(KQL + (tt * 4 + ks) * 1024 + l * 16);
  }
  if (w >= 4) wait_prog(rs, FL_QK + h * FS, 1, 1, l, qkdone, lim);
  u32x4 m = *(const u32x4*)(A.mem0 + (l & 7) * 512 + w * 64 + (l >> 3) * 8);
  u32x4 pa = m, pb = m, pc = m;
  int itc = 0;
  *(u32x4*)(memL + w * 1024 + l * 16) = m;
  const u32 memLds = (u32)(size_t)(__attribute__((address_space(3))) char*)(memL + w * 1024 + l * 16);
  for (int s = 0; s < 64; ++s) {
    char* kqb = KQL + (s & 1) * 16384;
    bar();
    float v[4];
    project(wf, memL, l, idx, v, [&]() {
      if (w >= 4 && s < 63 && !(MODEV & 32)) {
#pragma unroll
        for (int i = 0; i < 4; ++i) kq[i] = ld16(rs, kqOff + (u32)(s + 1) * (32 * 8192) + i * 64);
      }
    });
    {
      *(u32x2*)(qkL + (w * 32 + (g >> 1) * 16 + (l & 15)) * 16 + (g & 1) * 8) = u32x2{pk2(v[0], v[1]), pk2(v[2], v[3])};
    }
    if (isT1) drain();
    bar();
    if (w >= 4 && s < 63 && !(MODEV & 32)) poll_retire<4>(pa, pb, pc); else poll_retire<0>(pa, pb, pc);
    if (w == 3 && s >= 1 && l == 0) st4(rs, FL_XP + h * FS, (u32)(s - 1));
    if (w == 0 && s < 63) {
      u32x4 qf[4], mf[4];
#pragma unroll
      for (int ks = 0; ks < 4; ++ks) { qf[ks] = frag_tr<false>(qkL, 16, ks * 32, (l & 1) * 8, l); SB(); }
#pragma unroll
      for (int ks = 0; ks < 4; ++ks) { mf[ks] = frag_tr<false>(qkL + 2048, 16, ks * 32, (l & 1) * 8, l); SB(); }
      float ci = g >= 2 ? -1e30f : 0.f;
      f32x4 d0 = {ci, ci, ci, ci}, d1 = {0, 0, 0, 0}, d2 = {0, 0, 0, 0};
#pragma unroll
      for (int ks = 0; ks < 4; ++ks) { d1 = mma(aux[ks], qf[ks], d1); d2 = mma(aux[4 + ks], qf[ks], d2); SB(); }
      d0 = mma(mf[0], qf[0], d0); d0 = mma(mf[1], qf[1], d0); SB();
      float e0 = a ? d2[0] : d1[0], e1 = a ? d2[1] : d1[1], e2 = a ? d2[2] : d1[2], e3 = a ? d2[3] : d1[3];
      float m4 = fmaxf(fmaxf(e0, e1), fmaxf(e2, e3));
      SB();
      d0 = mma(mf[2], qf[2], d0); d0 = mma(mf[3], qf[3], d0); SB();
      float e4 = a ? d0[2] : d0[0], e5 = a ? d0[3] : d0[1];
      float mx = max3_red(m4, e4, e5);
      float mc = -mx * csc;
      e0 = __builtin_amdgcn_exp2f(fmaf(e0, csc, mc)); e1 = __builtin_amdgcn_exp2f(fmaf(e1, csc, mc));
      e2 = __builtin_amdgcn_exp2f(fmaf(e2, csc, mc)); e3 = __builtin_amdgcn_exp2f(fmaf(e3, csc, mc));
      e4 = __builtin_amdgcn_exp2f(fmaf(e4, csc, mc)); e5 = __builtin_amdgcn_exp2f(fmaf(e5, csc, mc));
      float sum = ((e0 + e1) + (e2 + e3)) + (e4 + e5);
      sum = red_sum4(sum);
      float rl = __builtin_amdgcn_rcpf(sum);
      rl = (s & 1) ? -rl : rl;
      u32x2 pt = {pk2(e0 * rl, e1 * rl), pk2(e2 * rl, e3 * rl)};
      u32 pm = pk2(e4 * rl, e5 * rl);
      const u32x4 gt = {lo8(pt.x), lo8(pt.y), hi8(pt.x), hi8(pt.y)};
      st16(rs, pTok, gt); st16(rs, pTok + 2 * rst, gt);
      const u32 ml = lo8(pm), mh = hi8(pm);
      auto sl = __builtin_amdgcn_permlane16_swap(ml, ml, false, false);
      auto sh = __builtin_amdgcn_permlane16_swap(mh, mh, false, false);
      if (l < 32) st16(rs, pMM, u32x4{sl[0], sh[0], sl[1], sh[1]});
    }
    if (isT1 && !(MODEV & 8)) {
      __builtin_amdgcn_s_sleep(3);
      u32x4 mf[4], kt[2][4], qt[4];
#pragma unroll
      for (int ks = 0; ks < 4; ++ks) {
        qt[ks] = *(const u32x4*)(kqb + (8 + tt1 * 4 + ks) * 1024 + l * 16);
        kt[0][ks] = *(const u32x4*)(kqb + ks * 1024 + l * 16); kt[1][ks] = *(const u32x4*)(kqb + (4 + ks) * 1024 + l * 16);
        mf[ks] = frag_tr<false>(qkL + 2048, 16, ks * 32, (l & 1) * 8, l);
      }
      f32x4 d0 = {0, 0, 0, 0}, d1 = {0, 0, 0, 0}, d2 = {0, 0, 0, 0};
#pragma unroll
      for (int ks = 0; ks < 4; ++ks) { d1 = mma(kt[0][ks], qt[ks], d1); d2 = mma(kt[1][ks], qt[ks], d2); }
#pragma unroll
      for (int ks = 0; ks < 4; ++ks) d0 = mma(mf[ks], qt[ks], d0);
      const int qi = tt1 * 16 + (l & 15);
      float e[12];
#pragma unroll
      for (int r = 0; r < 4; ++r) {
        e[r] = (4 * g + r <= qi) ? d1[r] : -1e30f;
        e[4 + r] = (16 + 4 * g + r <= qi) ? d2[r] : -1e30f;
        e[8 + r] = (g < 2) ? d0[r] : -1e30f;
      }
      float mx = e[0];
#pragma unroll
      for (int r = 1; r < 12; ++r) mx = fmaxf(mx, e[r]);
      mx = max1632(mx);
      float mc = -mx * csc, sum = 0.f;
#pragma unroll
      for (int r = 0; r < 12; ++r) { e[r] = __builtin_amdgcn_exp2f(fmaf(e[r], csc, mc)); sum += e[r]; }
      sum = sum1632(sum);
      float rl = __builtin_amdgcn_rcpf(sum);
      st16(rs, ptOff + (u32)s * 65536, nz4(u32x4{pk2(e[0] * rl, e[1] * rl), pk2(e[2] * rl, e[3] * rl), pk2(e[4] * rl, e[5] * rl), pk2(e[6] * rl, e[7] * rl)}));
      if (g < 2) st8(rs, pmOff + (u32)s * 16384, u32x2{nz(pk2(e[8] * rl, e[9] * rl)), nz(pk2(e[10] * rl, e[11] * rl))});
    }
    if (s < 63) {
      char* kqn = KQL + ((s + 1) & 1) * 16384;
      if (w >= 4) {
        __builtin_amdgcn_s_sleep(4);
        wait_prog(rs, FL_QK + h * FS, 1, min(s + 2, 63), l, qkdone, lim);
#pragma unroll
        for (int i = 0; i < 4; ++i) *(u32x4*)(kqn + ((w - 4) * 4 + i) * 1024 + l * 16) = kq[i];
      }
      bar();
      if (w == 0) {
#pragma unroll
        for (int tt = 0; tt < 2; ++tt)
#pragma unroll
          for (int ks = 0; ks < 4; ++ks) aux[tt * 4 + ks] = *(const u32x4*)(kqn + (tt * 4 + ks) * 1024 + l * 16);
      }
      poll_mem3<XGAP>(rs, memOff + ((s + 1) & 3) * 131072, memLds, lim, pa, pb, pc, itc);
    }
  }
  drain();
  bar();
  if (t == 192) st4(rs, FL_XP + h * FS, 63u);
}

#undef kq
DI void fold_task(const Args& A, char* smem, rsrc_t rs, int task) {
  const int t = threadIdx.x, l = t & 63, w = __builtin_amdgcn_readfirstlane(t >> 6);
  const int g = l >> 4, h = task >> 2, q = task & 3;
  const int r = l & 15, gr = r >> 2, rg = r & 3;
  const int drow = 64 * w + 16 * (gr >> 1) + 4 * (gr & 1) + 8 * (rg >> 1) + (rg & 1);
  const u16* Wv = A.Wv + (size_t)drow * 4096 + h * 128 + g * 8;
  u32x4 tmp[4], af[4][4];
  slab_load<16, 4>(A.Wo + (size_t)h * 128 * 512 + 128 * q, 512, t, tmp);
#pragma unroll
  for (int u = 0; u < 4; ++u)
#pragma unroll
    for (int ks = 0; ks < 4; ++ks) af[u][ks] = *(const u32x4*)(Wv + (size_t)(32 * (u >> 1) + 2 * (u & 1)) * 4096 + ks * 32);
  __builtin_amdgcn_sched_barrier(0);
  slab_store<16, 4>(smem, t, tmp);
  __syncthreads();
#pragma unroll
  for (int ct = 0; ct < 8; ++ct) {
    u32x4 wo[4];
#pragma unroll
    for (int ks = 0; ks < 4; ++ks) wo[ks] = frag_tr<false>(smem, 272, ks * 32, (16 * ct + 4 * (l & 3)) * 2, l);
    const int c = 128 * q + 16 * ct + r, cc = c >> 3, jj = c & 7;
#pragma unroll
    for (int half = 0; half < 2; ++half) {
      f32x4 a0 = {0, 0, 0, 0}, a1 = {0, 0, 0, 0};
#pragma unroll
      for (int ks = 0; ks < 4; ++ks) { a0 = mma(af[2 * half][ks], wo[ks], a0); a1 = mma(af[2 * half + 1][ks], wo[ks], a1); }
      const u32 o = (u32)((((((cc * 8 + (h >> 2)) * 2 + ((h >> 1) & 1)) * 8 + w) * 2 + half) * 64 + g * 16 + (h & 1) * 8 + jj)) * 16;
      st16(rs, OFF_BF + o, u32x4{pk2(a0[0], a0[1]), pk2(a0[2], a0[3]), pk2(a1[0], a1[1]), pk2(a1[2], a1[3])});
    }
  }
  drain();
  __syncthreads();
  if (t == 0) st4(rs, FL_FOLD + task * FS, 1u);
}

DI void ln_task(const Args& A, rsrc_t rs, int s0) {
  const int t = threadIdx.x, l = t & 63, w = __builtin_amdgcn_readfirstlane(t >> 6);
  const int g = l & 3, tk = (l >> 2) & 3, kq = l >> 4, tok = 4 * w + tk;
  u32x4 v[2][4];
#pragma unroll
  for (int u = 0; u < 2; ++u)
#pragma unroll
    for (int i = 0; i < 4; ++i) v[u][i] = *(const u32x4*)(A.x + (size_t)(32 * (s0 + 32 * u) + tok) * 512 + 32 * (4 * i + kq) + 8 * g);
#pragma unroll
  for (int u = 0; u < 2; ++u) {
    f32x2 c[16]; float var;
    ln_center(v[u], c, var);
    float r = __builtin_amdgcn_rsqf(var + 1e-5f);
#pragma unroll
    for (int i = 0; i < 4; ++i)
      st16(rs, OFF_XN + (u32)(s0 + 32 * u) * 32768 + (u32)((((tok >> 4) * 16 + 4 * i + kq) * 64) + g * 16 + ((tok & 15) ^ (4 * g))) * 16, ln_pack(c, i, r));
  }
  drain();
  __syncthreads();
  if (t < 2) st4(rs, FL_XN + (s0 + 32 * t) * FS, 1u);
}

DI void role_Y(const Args& A, char* smem, rsrc_t rs, int cc, int rep) {
  const int t = threadIdx.x, l = t & 63, w = __builtin_amdgcn_readfirstlane(t >> 6);
  const int g = l >> 4;
  int lim = SPIN_MAX;
  fold_task(A, smem, rs, (((cc & 3) + 4 * (cc >> 4)) << 2) | ((cc >> 2) & 3));
  for (int it = 0; it < lim; ++it) {
    int v = (int)ld4(rs, FL_FOLD + (u32)((4 * w + (l & 3)) * 4 + (cc >> 4)) * FS);
    if (__all(v >= 0)) break;
    __builtin_amdgcn_s_sleep(2);
  }
  u32x8 wf[2][8];
#pragma unroll
  for (int tt = 0; tt < 2; ++tt)
#pragma unroll
    for (int i = 0; i < 8; ++i) {
      int kc = i;
      u32 o = OFF_BF + (u32)((((cc * 8 + w) * 2 + tt) * 8 + kc) * 2) * 1024 + l * 16;
      u32x4 lo = ld16(rs, o), hi = ld16(rs, o + 1024);
      wf[tt][i] = u32x8{lo.x, lo.y, lo.z, lo.w, hi.x, hi.y, hi.z, hi.w};
    }
  wait_flags(rs, FL_SEL, 1, 0, l, lim);
  u32 sel = __builtin_amdgcn_readfirstlane(ld4(rs, FL_SEL)) & 1;
  if (MODEV & 1) sel = 0;
  if (MODEV & 2) sel = 1;
  char* memL = smem;
  char* YL = smem + 8192;
  float* RL = (float*)(smem + 12288);
  const int idx = (l & 8) ? 0xEEEE : 0x4444;
  const u32 commB = OFF_COMM + sel * 8192;
  const u32 memOff = commB + (PRM(rep) + 4) * 16384 + w * 1024 + l * 16;
  const int set = (l >> 3) & 1, r8 = l & 7;
  const u32 pBase = commB + (32 + (PLAY ? (w & 3) * 4 + PRMP(rep) : rep * 4 + (w & 3))) * 16384;
  const u32 pTok = pBase + set * 512 + (g * 8 + r8) * 16;
  const u32 pMM = pBase + 4096 + (set * 4 + g) * 128 + r8 * 16;
  const u32 uOff = OFF_U + (u32)cc * 16384 + (u32)(8 * (w & 3) + set) * 512 + g * 128 + r8 * 16;
  char* UL = smem + 16384;
  int vudone = -1;
  u32x4 un[4];
  if (w >= 4) {
    wait_prog(rs, FL_VU, 32, 0, l, vudone, lim);
#pragma unroll
    for (int mm = 0; mm < 4; ++mm) un[mm] = ld16(rs, uOff + mm * 1024);
  }
  u32x4 m = *(const u32x4*)(A.mem0 + (l & 7) * 512 + w * 64 + (l >> 3) * 8);
  const int rlIdx = (l & 7) * 8 + ((l >> 4) & 1) + 2 * (l >> 5) + 4 * ((l >> 3) & 1);
  const u32 pubOff = commB + (l >> 3) * 16384 + cc * 128 + (l & 7) * 16;
  if (w == 4) {
#pragma unroll
    for (int i = 0; i < 4; ++i) {
      int id = i * 64 + l;
      st16(rs, OFF_YP + (u32)((63 * 32 + (id >> 3)) * 512 + cc * 8 + (id & 7)) * 16, u32x4{SENT, SENT, SENT, SENT});
    }
  }
  u32 tref = now32();
  int dP = DYV;
  for (int s = 0; s < 64; ++s) {
    *(u32x4*)(memL + w * 1024 + l * 16) = m;
    char* ub = UL + (s & 1) * 16384 + (w & 3) * 4096 + l * 16;
    if (w >= 4) {
#pragma unroll
      for (int mm = 0; mm < 4; ++mm) *(u32x4*)(ub + mm * 1024) = un[mm];
    }
    bar();
    if (w >= 4 && s < 62 && !(MODEV & 16)) {
      wait_prog(rs, FL_VU, 32, s + 1, l, vudone, lim);
#pragma unroll
      for (int mm = 0; mm < 4; ++mm) un[mm] = ld16(rs, uOff + (u32)(s + 1) * (64 * 16384) + mm * 1024);
    }
    if (w == 5 && s >= 2)
      st16(rs, commB + (((s - 1) & 3) * 8 + (l >> 3)) * 16384 + cc * 128 + (l & 7) * 16, u32x4{0xFFFFFFFFu, 0xFFFFFFFFu, 0xFFFFFFFFu, 0xFFFFFFFFu});
    float v[4];
    u32x4 uf[4];
    project(wf, memL, l, idx, v, [&]() {
      if (w < 4) {
#pragma unroll
        for (int mm = 0; mm < 4; ++mm) uf[mm] = *(const u32x4*)(ub + mm * 1024);
      }
    });
    {
      u32x2 pk = {pk2(v[0], v[1]), pk2(v[2], v[3])};
      int head = 4 * w + 2 * (g >> 1) + ((l >> 3) & 1);
      *(u32x2*)(YL + (head * 8 + (l & 7)) * 16 + (g & 1) * 8) = pk;
    }
    bar();
    if (w >= 5 && s < 61) wait_prog(rs, FL_VU, 32, s + 2, l, vudone, lim);
    if (w == 4 && !(MODEV & 128)) {
#pragma unroll
      for (int i = 0; i < 4; ++i) {
        int id = i * 64 + l;
        u32x4 y = *(const u32x4*)(YL + id * 16);
        st16(rs, OFF_YP + (u32)((s * 32 + (id >> 3)) * 512 + cc * 8 + (id & 7)) * 16, nz4(y));
      }
    }
    if (w == 4 && s < 61) wait_prog(rs, FL_VU, 32, s + 2, l, vudone, lim);
    if (s < 63) {
      if (w < 4) {
        u32x4 yf = *(const u32x4*)(YL + ((8 * w + 4 * set + g) * 8 + r8) * 16);
        u32x4 pf[5];
        wait_for(tref, dP);
        const u32 tm = (s & 1) ? 0x80008000u : 0u;
        bool ok0 = false, ok1 = false, ok2 = false, ok3 = false, ok4 = false;
        int it = 0;
        for (; it < lim; ++it) {
          if (!ok0) pf[0] = ld16(rs, pTok);
          if (!ok1) pf[1] = ld16(rs, pTok + 1024);
          if (!ok2) pf[2] = ld16(rs, pTok + 2048);
          if (!ok3) pf[3] = ld16(rs, pTok + 3072);
          if (!ok4) pf[4] = ld16(rs, pMM);
          ok0 = (((pf[0].x ^ tm) | (pf[0].z ^ tm)) & 0x80008000u) == 0;
          ok1 = (((pf[1].x ^ tm) | (pf[1].z ^ tm)) & 0x80008000u) == 0;
          ok2 = (((pf[2].x ^ tm) | (pf[2].z ^ tm)) & 0x80008000u) == 0;
          ok3 = (((pf[3].x ^ tm) | (pf[3].z ^ tm)) & 0x80008000u) == 0;
          ok4 = (((pf[4].x ^ tm) | (pf[4].y ^ tm) | (pf[4].z ^ tm) | (pf[4].w ^ tm)) & 0x80008000u) == 0;
          if (__all(ok0 && ok1 && ok2 && ok3 && ok4)) break;
          asm volatile("" ::: "memory");
        }
        if (it >= lim) lim = 1;
        dP += (it == 0) ? -ADEC : min(it, 4) * AINC;
        dP = max(min(dP, 6000), 0);
        f32x4 d = {0, 0, 0, 0}, d2 = {0, 0, 0, 0};
        d = mma(pf[0], uf[0], d); d2 = mma(pf[1], uf[1], d2); d = mma(pf[2], uf[2], d); d2 = mma(pf[3], uf[3], d2); d = mma(pf[4], yf, d);
        d += d2;
        if ((g >> 1) == set) {
          float* dst = RL + (w * 2 + set) * 64 + (4 * (g & 1)) * 8 + r8;
          dst[0] = d[0]; dst[8] = d[1]; dst[16] = d[2]; dst[24] = d[3];
        }
      }
      if (w == 4) drain();
      bar();
      if (w == 0) {
        const float* R = RL + rlIdx;
        float x0 = R[0], x1 = R[64], x2 = R[128], x3 = R[192], x4 = R[256], x5 = R[320], x6 = R[384], x7 = R[448];
        float sum = ((x0 + x1) + (x2 + x3)) + ((x4 + x5) + (x6 + x7));
        auto s16 = __builtin_amdgcn_permlane16_swap(__float_as_uint(sum), __float_as_uint(sum), false, false);
        u32 pk = pk2(__uint_as_float(s16[0]), __uint_as_float(s16[1]));
        if (s & 1) pk ^= 0x80008000u;
        pk = min(pk, 0xFFFFFFFEu);
        auto s32 = __builtin_amdgcn_permlane32_swap(pk, pk, false, false);
        u32x4 o = {lo8(s32[0]), lo8(s32[1]), hi8(s32[0]), hi8(s32[1])};
        if (l < 32 || YREP) st16(rs, pubOff + ((s + 1) & 3) * 131072, o);
      }
      const u32 tpub = now32();
      wait_for(tpub, DYMV);
      m = poll_mem(rs, memOff + ((s + 1) & 3) * 131072, lim);
      tref = now32();
      drain();
    }
  }
}

DI void role_QK(const Args& A, char* smem, rsrc_t rs, int h) {
  const int t = threadIdx.x, l = t & 63, w = __builtin_amdgcn_readfirstlane(t >> 6);
  const int g = l >> 4;
  int lim = SPIN_MAX;
  u32x4 wf[2][16];
  {
    u32x4 tmp[2][16];
#pragma unroll
    for (int rnd = 0; rnd < 2; ++rnd)
#pragma unroll
      for (int i = 0; i < 16; ++i) {
        int id = i * 512 + t, row = id >> 5, c8 = id & 31;
        tmp[rnd][i] = *(const u32x4*)((c8 < 16 ? A.Wq : A.Wk) + (size_t)(rnd * 256 + row) * 4096 + h * 128 + (c8 & 15) * 8);
      }
    SB();
#pragma unroll
    for (int rnd = 0; rnd < 2; ++rnd) {
      slab_store<32, 16>(smem, t, tmp[rnd]);
      __syncthreads();
#pragma unroll
      for (int ks = 0; ks < 8; ++ks)
#pragma unroll
        for (int te = 0; te < 2; ++te) wf[te][rnd * 8 + ks] = frag_tr<false>(smem, 528, ks * 32, (32 * w + 8 * (l & 3) + 4 * te) * 2, l);
      __syncthreads();
    }
  }
  if (w == 0) wait_flags(rs, FL_XN, 8, 0, l, lim);
  __syncthreads();
  u32x4 pf[4];
#pragma unroll
  for (int i = 0; i < 4; ++i) pf[i] = ld16(rs, OFF_XN + (i * 512 + t) * 16);
#pragma unroll
  for (int i = 0; i < 4; ++i) *(u32x4*)(smem + (i * 512 + t) * 16) = pf[i];
  bar();
  const int slot = (g * 16 + ((l & 15) ^ (4 * g))) * 16;
  const u32 oOff = (w < 4 ? OFF_QX : OFF_KX) + (u32)h * 8192 + (l & 15) * 256 + ((w & 3) * 32 + 8 * g) * 2;
  for (int s = 0; s < 64; ++s) {
    if (s == 6) { if (w == 0) wait_flags(rs, FL_XN, 64, 0, l, lim); bar(); }
#pragma unroll
    for (int i = 0; i < 4; ++i) pf[i] = ld16(rs, OFF_XN + (u32)min(s + 1, 63) * 32768 + (i * 512 + t) * 16);
    __builtin_amdgcn_sched_barrier(0);
    const char* xb = smem + (s & 1) * 32768 + slot;
    f32x4 acc[2][2] = {};
    u32x4 bb[2][4][2];
#pragma unroll
    for (int k = 0; k < 4; ++k) { bb[0][k][0] = *(const u32x4*)(xb + k * 1024); bb[0][k][1] = *(const u32x4*)(xb + 16384 + k * 1024); }
#pragma unroll
    for (int gq = 0; gq < 4; ++gq) {
      if (gq < 3) {
#pragma unroll
        for (int k = 0; k < 4; ++k) {
          bb[(gq + 1) & 1][k][0] = *(const u32x4*)(xb + ((gq + 1) * 4 + k) * 1024);
          bb[(gq + 1) & 1][k][1] = *(const u32x4*)(xb + 16384 + ((gq + 1) * 4 + k) * 1024);
        }
      }
      __builtin_amdgcn_sched_barrier(0);
#pragma unroll
      for (int k = 0; k < 4; ++k) {
        const int ks = gq * 4 + k;
        acc[0][0] = mma(wf[0][ks], bb[gq & 1][k][0], acc[0][0]); acc[1][0] = mma(wf[1][ks], bb[gq & 1][k][0], acc[1][0]);
        acc[0][1] = mma(wf[0][ks], bb[gq & 1][k][1], acc[0][1]); acc[1][1] = mma(wf[1][ks], bb[gq & 1][k][1], acc[1][1]);
      }
      __builtin_amdgcn_sched_barrier(0);
    }
    drain();
#pragma unroll
    for (int i = 0; i < 4; ++i) *(u32x4*)(smem + ((s + 1) & 1) * 32768 + (i * 512 + t) * 16) = pf[i];
#pragma unroll
    for (int tk = 0; tk < 2; ++tk)
      st16(rs, oOff + (u32)s * (32 * 8192) + tk * 4096,
           u32x4{pk2(acc[0][tk][0], acc[0][tk][1]), pk2(acc[0][tk][2], acc[0][tk][3]), pk2(acc[1][tk][0], acc[1][tk][1]), pk2(acc[1][tk][2], acc[1][tk][3])});
    if (s <= 2 || s == 63) drain();
    bar();
    if (t == 0) st4(rs, FL_QK + h * FS, (u32)((s <= 2 || s == 63) ? s : s - 1));
  }
}

DI void role_VU(const Args& A, char* smem, rsrc_t rs, int h) {
  const int t = threadIdx.x, l = t & 63, w = __builtin_amdgcn_readfirstlane(t >> 6);
  const int g = l >> 4;
  int lim = SPIN_MAX;
  u32x4 wv[16], wo[4][4];
  {
    u32x4 tmp[16];
    slab_load<16, 16>(A.Wv + h * 128, 4096, t, tmp);
    slab_store<16, 16>(smem, t, tmp);
    __syncthreads();
    slab_load<64, 16>(A.Wo + (size_t)h * 128 * 512, 512, t, tmp);
#pragma unroll
    for (int ks = 0; ks < 16; ++ks) wv[ks] = frag_tr<false>(smem, 272, ks * 32, (16 * w + 4 * (l & 3)) * 2, l);
    __syncthreads();
    slab_store<64, 16>(smem, t, tmp);
    __syncthreads();
#pragma unroll
    for (int ct = 0; ct < 4; ++ct)
#pragma unroll
      for (int ks = 0; ks < 4; ++ks) wo[ct][ks] = frag_tr<true>(smem, 1040, ks * 32, (64 * w + 16 * ct + 4 * (l & 3)) * 2, l);
    __syncthreads();
  }
  if (w == 0) wait_flags(rs, FL_XN, 8, 0, l, lim);
  __syncthreads();
  u32x4 pf[4];
#pragma unroll
  for (int i = 0; i < 4; ++i) pf[i] = ld16(rs, OFF_XN + (i * 512 + t) * 16);
#pragma unroll
  for (int i = 0; i < 4; ++i) *(u32x4*)(smem + (i * 512 + t) * 16) = pf[i];
  bar();
  char* VL = smem + 32768;
  const int slot = (g * 16 + ((l & 15) ^ (4 * g))) * 16;
  const u32 uOff = OFF_U + (u32)(8 * w + ((l >> 3) & 1)) * 16384 + (u32)h * 512 + g * 128 + (l & 7) * 16;
  for (int s = 0; s < 64; ++s) {
    if (s == 6) { if (w == 0) wait_flags(rs, FL_XN, 64, 0, l, lim); bar(); }
#pragma unroll
    for (int i = 0; i < 4; ++i) pf[i] = ld16(rs, OFF_XN + (u32)min(s + 1, 63) * 32768 + (i * 512 + t) * 16);
    __builtin_amdgcn_sched_barrier(0);
    const char* xb = smem + slot;
    f32x4 av[2] = {};
    u32x4 bb[2][4][2];
#pragma unroll
    for (int k = 0; k < 4; ++k) { bb[0][k][0] = *(const u32x4*)(xb + k * 1024); bb[0][k][1] = *(const u32x4*)(xb + 16384 + k * 1024); }
#pragma unroll
    for (int gq = 0; gq < 4; ++gq) {
      if (gq < 3) {
#pragma unroll
        for (int k = 0; k < 4; ++k) {
          bb[(gq + 1) & 1][k][0] = *(const u32x4*)(xb + ((gq + 1) * 4 + k) * 1024);
          bb[(gq + 1) & 1][k][1] = *(const u32x4*)(xb + 16384 + ((gq + 1) * 4 + k) * 1024);
        }
      }
      __builtin_amdgcn_sched_barrier(0);
#pragma unroll
      for (int k = 0; k < 4; ++k) { av[0] = mma(wv[gq * 4 + k], bb[gq & 1][k][0], av[0]); av[1] = mma(wv[gq * 4 + k], bb[gq & 1][k][1], av[1]); }
      __builtin_amdgcn_sched_barrier(0);
    }
#pragma unroll
    for (int tk = 0; tk < 2; ++tk)
      *(u32x2*)(VL + (tk * 4 + (w >> 1)) * 1024 + l * 16 + (w & 1) * 8) = u32x2{pk2(av[tk][0], av[tk][1]), pk2(av[tk][2], av[tk][3])};
    bar();
    u32x4 vf[2][4];
#pragma unroll
    for (int tk = 0; tk < 2; ++tk)
#pragma unroll
      for (int ks = 0; ks < 4; ++ks) vf[tk][ks] = *(const u32x4*)(VL + (tk * 4 + ks) * 1024 + l * 16);
    drain();
#pragma unroll
    for (int i = 0; i < 4; ++i) *(u32x4*)(smem + (i * 512 + t) * 16) = pf[i];
#pragma unroll
    for (int ct = 0; ct < 4; ++ct) {
      f32x4 a0 = {0, 0, 0, 0}, a1 = {0, 0, 0, 0};
#pragma unroll
      for (int ks = 0; ks < 4; ++ks) { a0 = mma(vf[0][ks], wo[ct][ks], a0); a1 = mma(vf[1][ks], wo[ct][ks], a1); }
      st16(rs, uOff + (u32)s * (64 * 16384) + ct * 2 * 16384, u32x4{pk2(a0[0], a0[1]), pk2(a0[2], a0[3]), pk2(a1[0], a1[1]), pk2(a1[2], a1[3])});
    }
    if (s <= 2 || s == 63) drain();
    bar();
    if (t == 0) st4(rs, FL_VU + h * FS, (u32)((s <= 2 || s == 63) ? s : s - 1));
  }
}

DI f32x4 reduce8(char* smem, const f32x4 (&acc)[4][2], int w, int l) {
#pragma unroll
  for (int ct = 0; ct < 4; ++ct)
#pragma unroll
    for (int tk = 0; tk < 2; ++tk) *(f32x4*)(smem + ((w * 8 + ct * 2 + tk) * 64 + l) * 16) = acc[ct][tk];
  bar();
  f32x4 p[8];
#pragma unroll
  for (int i = 0; i < 8; ++i) p[i] = *(const f32x4*)(smem + ((i * 8 + w) * 64 + l) * 16);
  return ((p[0] + p[1]) + (p[2] + p[3])) + ((p[4] + p[5]) + (p[6] + p[7]));
}

DI void role_T2(const Args& A, char* smem, rsrc_t rs, int gi, int j) {
  const int t = threadIdx.x, l = t & 63, w = __builtin_amdgcn_readfirstlane(t >> 6);
  const int g = l >> 4, r = l & 15;
  int lim = SPIN_MAX, vudone = -1;
  const u32 uOff = OFF_U + (u32)(8 * j + (r >> 2)) * 16384 + (u32)(4 * w) * 512 + g * 128 + (r & 3) * 16;
  const u32 yOff = OFF_YP + (u32)((4 * w + g) * 512 + 64 * j + 8 * (r >> 2) + (r & 3)) * 16;
  const int ct = w >> 1, tk = w & 1;
  const int ecol = 64 * j + 32 * (ct >> 1) + 8 * g + 4 * (ct & 1);
  if (gi == 3) st8(rs, OFF_H + (u32)(ecol >> 5) * KBS + (u32)(32 * 63 + 16 * tk + r) * 64 + (ecol & 31) * 2, u32x2{SENT, SENT});
  for (int s = gi; s < 64; s += 4) {
    if (vudone < s) { if (w == 0) wait_prog(rs, FL_VU, 32, s, l, vudone, lim); bar(); vudone = s; if (w == 0) vudone = s; }
    u32x4 uf[4][4];
#pragma unroll
    for (int hh = 0; hh < 4; ++hh)
#pragma unroll
      for (int c = 0; c < 4; ++c) uf[hh][c] = ld16(rs, uOff + (u32)s * (64 * 16384) + (c >> 1) * 4 * 16384 + (c & 1) * 64 + hh * 512);
    const int tok = 32 * s + 16 * tk + r;
    u32x2 xv = *(const u32x2*)(A.x + (size_t)tok * 512 + ecol);
    if (w == 0) wait_flags_bk(rs, FL_XP, 32, min(s, 62), l, lim);
    bar();
    u32x4 pf[4][2], pm[2], yf[4];
    for (int it = 0;; ++it) {
#pragma unroll
      for (int hh = 0; hh < 4; ++hh)
#pragma unroll
        for (int k = 0; k < 2; ++k) pf[hh][k] = ld16(rs, OFF_PT + (u32)((s * 32 + 4 * w + hh) * 2 + k) * 1024 + l * 16);
#pragma unroll
      for (int k = 0; k < 2; ++k) pm[k] = ld16(rs, OFF_PM + (u32)((s * 8 + w) * 2 + k) * 1024 + l * 16);
#pragma unroll
      for (int c = 0; c < 4; ++c) yf[c] = ld16(rs, yOff + (u32)s * (32 * 512 * 16) + ((c >> 1) * 32 + (c & 1) * 4) * 16);
      SB();
      if (s != 63) break;
      u32 mx = max(mx4(pm[0]), mx4(pm[1]));
#pragma unroll
      for (int c = 0; c < 4; ++c) mx = max(max(mx, mx4(yf[c])), max(mx4(pf[c][0]), mx4(pf[c][1])));
      if (__all(mx != SENT) || it >= lim) break;
      MEMF();
    }
    f32x4 acc[4][2] = {};
#pragma unroll
    for (int hh = 0; hh < 4; ++hh)
#pragma unroll
      for (int c = 0; c < 4; ++c) { acc[c][0] = mma(uf[hh][c], pf[hh][0], acc[c][0]); acc[c][1] = mma(uf[hh][c], pf[hh][1], acc[c][1]); }
#pragma unroll
    for (int c = 0; c < 4; ++c) { acc[c][0] = mma(yf[c], pm[0], acc[c][0]); acc[c][1] = mma(yf[c], pm[1], acc[c][1]); }
    f32x4 o = reduce8(smem, acc, w, l);
    u32 a0 = pk2(o[0], o[1]), a1 = pk2(o[2], o[3]);
    u32x2 hv = {nz(pk2(lo16(xv.x) + lo16(a0), hi16(xv.x) + hi16(a0))), nz(pk2(lo16(xv.y) + lo16(a1), hi16(xv.y) + hi16(a1)))};
    st8(rs, OFF_H + (u32)(ecol >> 5) * KBS + (u32)tok * 64 + (ecol & 31) * 2, hv);
    drain();
    bar();
    if (t == 0) st4(rs, FL_T2 + (gi * 8 + j) * FS, (u32)s);
  }
}

template <int MODE>
DI void role_B128(const Args& A, char* smem, rsrc_t rs, int gi, int j, u32 t0) {
  const int t = threadIdx.x, l = t & 63, w = __builtin_amdgcn_readfirstlane(t >> 6);
  const int g = l >> 4, r = l & 15, cg = w & 3, tk = w >> 2;
  int lim = SPIN_MAX;
  u32x4 wf[2][16];
  if (MODE != 0 || gi == 1) {
    const u32 sl = MODE == 0 ? 63 : 62 + gi;
    st16(rs, (MODE == 0 ? OFF_H1 : MODE == 1 ? OFF_X1 : OFF_F) + (u32)(4 * j + cg) * KBS + (u32)(32 * sl + 16 * tk + r) * 64 + g * 16, u32x4{SENT, SENT, SENT, SENT});
    if (MODE == 0) st16(rs, OFF_YB + (u32)(4 * j + (l >> 4)) * KBS + (u32)(32 * 63 + 4 * w + ((l >> 2) & 3)) * 64 + (l & 3) * 16, u32x4{SENT, SENT, SENT, SENT});
  }
  hold(t0, MODE == 0 ? BD0 : BD1);
  {
    u32x4 tmp[16];
    const u16* W = (MODE == 0 ? A.w1 : MODE == 1 ? A.w2 : A.w3) + 128 * j;
    slab_load<16, 16>(W, MODE == 2 ? 1792 : 512, t, tmp);
    slab_store<16, 16>(smem, t, tmp);
    __syncthreads();
#pragma unroll
    for (int ks = 0; ks < 16; ++ks)
#pragma unroll
      for (int te = 0; te < 2; ++te) wf[te][ks] = frag_tr<false>(smem, 272, ks * 32, (32 * cg + 8 * (l & 3) + 4 * te) * 2, l);
    __syncthreads();
  }
  const int lg = l & 3, ltk = (l >> 2) & 3, lkq = l >> 4, ltok = 4 * w + ltk;
  const int rslot = (MODE == 1) ? l * 16 : (g * 16 + (r ^ (4 * g))) * 16;
  for (int s = gi; s < 64; s += 2) {
    u32x4 yv;
    if (MODE == 1) {
      if (w == 0) {
        if (s < 62) {
          wait_flags(rs, FL_B1 + (s & 1) * 4 * FS, 4, s, l, lim, SLPV);
          wait_flags(rs, FL_B1 + ((s + 1) & 1) * 4 * FS, 4, s + 1, l, lim, SLPV);
        } else {
          wait_flags(rs, FL_B1, 4, 62, l, lim, SLPV);
          wait_flags(rs, FL_T2 + 3 * 8 * FS, 8, 63, l, lim, SLPV);
        }
      }
      bar();
      u32x4 v[4];
      for (int it = 0;; ++it) {
#pragma unroll
        for (int i = 0; i < 4; ++i) {
          const int f = 4 * w + i, ks = f & 15, grp = ks >> 2;
          const int sh = (grp == 0) ? -1 : (grp == 1) ? 1 : (grp == 2) ? -2 : 2;
          const int row = 32 * s + 16 * (f >> 4) + r + sh;
          const bool okr = row >= 0 && row < 2048;
          u32x4 z = ld16(rs, OFF_H1 + (u32)ks * KBS + (u32)(okr ? row : 0) * 64 + g * 16);
          v[i] = okr ? z : u32x4{0, 0, 0, 0};
        }
        yv = ld16(rs, OFF_YB + (u32)(4 * j + cg) * KBS + (u32)(32 * s + 16 * tk + r) * 64 + g * 16);
        SB();
        if (s < 62) break;
        const u32 mx = max(max(max(mx4(v[0]), mx4(v[1])), max(mx4(v[2]), mx4(v[3]))), mx4(yv));
        if (__all(mx != SENT) || it >= lim) break;
        MEMF();
      }
#pragma unroll
      for (int i = 0; i < 4; ++i) *(u32x4*)(smem + (4 * w + i) * 1024 + l * 16) = v[i];
    } else {
      const bool last = s >= (MODE == 0 ? 63 : 62);
      if (w == 0) {
        if (MODE == 0) { if (last) wait_flags(rs, FL_XP, 32, 63, l, lim, SLPV); else wait_flags(rs, FL_T2 + (s & 3) * 8 * FS, 8, s, l, lim, SLPV); }
        else { if (last) wait_flags(rs, FL_B1 + 4 * FS, 4, 63, l, lim, SLPV); else wait_flags(rs, FL_B2A + gi * 4 * FS, 4, s, l, lim, SLPV); }
      }
      bar();
      u32x4 v[4];
      for (int it = 0;; ++it) {
#pragma unroll
        for (int i = 0; i < 4; ++i) v[i] = ld16(rs, (MODE == 0 ? OFF_H : OFF_X1) + (u32)(4 * i + lkq) * KBS + (u32)(32 * s + ltok) * 64 + lg * 16);
        SB();
        if (!last) break;
        const u32 mx = max(max(mx4(v[0]), mx4(v[1])), max(mx4(v[2]), mx4(v[3])));
        if (__all(mx != SENT) || it >= lim) break;
        MEMF();
      }
      f32x2 c[16]; float var;
      ln_center(v, c, var);
      float r1 = __builtin_amdgcn_rsqf(var + 1e-5f), rz = r1;
      if (MODE == 0) {
        rz = r1 * __builtin_amdgcn_rsqf(var * r1 * r1 + 1e-5f);
#pragma unroll
        for (int i = 0; i < 4; ++i)
          if (i == j) st16(rs, OFF_YB + (u32)(4 * i + lkq) * KBS + (u32)(32 * s + ltok) * 64 + lg * 16, nz4(ln_pack(c, i, r1)));
      }
#pragma unroll
      for (int i = 0; i < 4; ++i)
        *(u32x4*)(smem + (((ltok >> 4) * 16 + 4 * i + lkq) * 64 + lg * 16 + ((ltok & 15) ^ (4 * lg))) * 16) = ln_pack(c, i, rz);
    }
    bar();
    f32x4 a0 = {0, 0, 0, 0}, a1 = {0, 0, 0, 0};
    const char* xb = smem + tk * 16384 + rslot;
#pragma unroll
    for (int ks = 0; ks < 16; ++ks) {
      u32x4 b = *(const u32x4*)(xb + ks * 1024);
      a0 = mma(wf[0][ks], b, a0); a1 = mma(wf[1][ks], b, a1);
    }
    u32x4 o;
    if (MODE == 1) {
      u32 p0 = pk2(a0[0], a0[1]), p1 = pk2(a0[2], a0[3]), p2 = pk2(a1[0], a1[1]), p3 = pk2(a1[2], a1[3]);
      o = u32x4{pk2(lo16(yv.x) + lo16(p0), hi16(yv.x) + hi16(p0)), pk2(lo16(yv.y) + lo16(p1), hi16(yv.y) + hi16(p1)),
                pk2(lo16(yv.z) + lo16(p2), hi16(yv.z) + hi16(p2)), pk2(lo16(yv.w) + lo16(p3), hi16(yv.w) + hi16(p3))};
    } else {
      o = u32x4{pk2(gelu_t(a0[0]), gelu_t(a0[1])), pk2(gelu_t(a0[2]), gelu_t(a0[3])), pk2(gelu_t(a1[0]), gelu_t(a1[1])), pk2(gelu_t(a1[2]), gelu_t(a1[3]))};
    }
    st16(rs, (MODE == 0 ? OFF_H1 : MODE == 1 ? OFF_X1 : OFF_F) + (u32)(4 * j + cg) * KBS + (u32)(32 * s + 16 * tk + r) * 64 + g * 16, nz4(o));
    drain();
    bar();
    if (t == 0) st4(rs, (MODE == 0 ? FL_B1 + gi * 4 * FS : MODE == 1 ? FL_B2A + gi * 4 * FS : FL_B2B + gi * 16 * FS) + j * FS, (u32)s);
  }
}

DI void role_B2c(const Args& A, char* smem, rsrc_t rs, int gi, int j, u32 t0) {
  const int t = threadIdx.x, l = t & 63, w = __builtin_amdgcn_readfirstlane(t >> 6);
  const int g = l >> 4, r = l & 15;
  int lim = SPIN_MAX;
  u32x4 wf[4][7];
  hold(t0, BD1);
#pragma unroll
  for (int rnd = 0; rnd < 2; ++rnd) {
    u32x4 tmp[14];
    slab_load<8, 14>(A.w4 + (size_t)rnd * 896 * 512 + 64 * j, 512, t, tmp);
    slab_store<8, 14>(smem, t, tmp);
    __syncthreads();
    if ((w >> 2) == rnd) {
#pragma unroll
      for (int ks = 0; ks < 7; ++ks)
#pragma unroll
        for (int c = 0; c < 4; ++c) wf[c][ks] = frag_tr<false>(smem, 144, (w & 3) * 224 + ks * 32, (32 * (c >> 1) + 8 * (l & 3) + 4 * (c & 1)) * 2, l);
    }
    __syncthreads();
  }
  const int ct = w >> 1, tk = w & 1;
  const int ecol = 64 * j + 32 * (ct >> 1) + 8 * g + 4 * (ct & 1);
  for (int s = gi; s < 64; s += 2) {
    if (w == 0) {
      if (s >= 62) wait_flags(rs, FL_B2A + gi * 4 * FS, 4, s, l, lim, SLPV);
      else wait_flags(rs, FL_B2B + gi * 16 * FS, 14, s, l, lim, SLPV);
    }
    bar();
    u32x4 ff[2][7];
    const int tok = 32 * s + 16 * tk + r;
    const u32 eo = (u32)(ecol >> 5) * KBS + (u32)tok * 64 + (ecol & 31) * 2;
    u32x2 hv, xv;
    for (int it = 0;; ++it) {
#pragma unroll
      for (int k = 0; k < 2; ++k)
#pragma unroll
        for (int ks = 0; ks < 7; ++ks) ff[k][ks] = ld16(rs, OFF_F + (u32)(7 * w + ks) * KBS + (u32)(32 * s + 16 * k + r) * 64 + g * 16);
      hv = ld8(rs, OFF_H + eo); xv = ld8(rs, OFF_X1 + eo);
      SB();
      if (s < 62) break;
      u32 mx = 0;
#pragma unroll
      for (int ks = 0; ks < 7; ++ks) mx = max(mx, max(mx4(ff[0][ks]), mx4(ff[1][ks])));
      if (__all(mx != SENT) || it >= lim) break;
      MEMF();
    }
    f32x4 acc[4][2] = {};
#pragma unroll
    for (int ks = 0; ks < 7; ++ks)
#pragma unroll
      for (int c = 0; c < 4; ++c) { acc[c][0] = mma(wf[c][ks], ff[0][ks], acc[c][0]); acc[c][1] = mma(wf[c][ks], ff[1][ks], acc[c][1]); }
    f32x4 o = reduce8(smem, acc, w, l);
    u32 a0 = pk2(o[0], o[1]), a1 = pk2(o[2], o[3]);
    u32 b0 = pk2(lo16(xv.x) + lo16(a0), hi16(xv.x) + hi16(a0)), b1 = pk2(lo16(xv.y) + lo16(a1), hi16(xv.y) + hi16(a1));
    u32x2 ov = {pk2(lo16(hv.x) + lo16(b0), hi16(hv.x) + hi16(b0)), pk2(lo16(hv.y) + lo16(b1), hi16(hv.y) + hi16(b1))};
    *(u32x2*)(A.out + (size_t)tok * 512 + ecol) = ov;
    bar();
  }
}

DI void calib(rsrc_t rs, char* ws) {
  if (threadIdx.x == 0) {
    u32 res0 = 0xFFFFFFFFu, res1 = 0xFFFFFFFFu;
    for (int rep = 0; rep < 10; ++rep) {
      u32* q = (u32*)(ws + OFF_COMM + (rep & 1) * 8192); u32 v = 0xFFFFFFFFu;
      asm volatile("global_store_dword %0, %1, off sc1\n\ts_waitcnt vmcnt(0)" :: "v"(q), "v"(v) : "memory");
      unsigned long long c1 = __builtin_amdgcn_s_memtime(); u32 r;
      asm volatile("global_load_dword %0, %1, off sc1\n\ts_waitcnt vmcnt(0)" : "=v"(r) : "v"(q) : "memory");
      unsigned long long c2 = __builtin_amdgcn_s_memtime();
      u32 d = (u32)(c2 - c1) + (r & 0);
      if (rep & 1) res1 = min(res1, d); else res0 = min(res0, d);
    }
    st4(rs, FL_SEL, res0 < res1 ? 0u : 1u);
  }
}

__global__ __launch_bounds__(512, 2) void k_mega(Args A) {
  extern __shared__ __attribute__((aligned(16))) char smem[];
  const int b = blockIdx.x, xcd = b & 7, id = (b >> 3) * 4 + (xcd & 3);
  rsrc_t rs = __builtin_amdgcn_make_buffer_rsrc((void*)A.ws, 0, WS_USED, 0x00020000);
  const u32 t0 = (u32)__builtin_amdgcn_s_memrealtime();
  if (xcd < 4) {
    if (id < 32) role_X(A, smem, rs, id, xcd, t0);
    else if (id < 96) role_Y(A, smem, rs, id - 32, xcd);
    else {
      const int a = id - 96;
      if (a == 8) calib(rs, A.ws);
      ln_task(A, rs, a);
      if (MODEV & 64) return;
      if (a < 8) role_B128<0>(A, smem, rs, a >> 2, a & 3, t0);
      else if (a < 16) role_B128<1>(A, smem, rs, (a - 8) >> 2, a & 3, t0);
      else role_B2c(A, smem, rs, (a - 16) >> 3, a & 7, t0);
    }
  } else {
    if (id < 32) role_QK(A, smem, rs, id);
    else if (id < 64) role_VU(A, smem, rs, id - 32);
    else {
      fold_task(A, smem, rs, ((16 + (id & 3) + 4 * ((id - 64) >> 4)) << 2) | ((id >> 2) & 3));
      if (MODEV & 64) return;
      if (id < 96) role_T2(A, smem, rs, (id - 64) >> 3, id & 7);
      else if (id < 124) role_B128<2>(A, smem, rs, (id - 96) / 14, (id - 96) % 14, t0);
    }
  }
}

extern "C" void kernel_launch(void* const* d_in, const int* in_sizes, int n_in, void* d_out, int out_size, void* d_ws, size_t ws_size, hipStream_t stream) {
  Args A;
  A.x = (const u16*)d_in[0]; A.mem0 = (const u16*)d_in[1];
  A.Wq = (const u16*)d_in[2]; A.Wk = (const u16*)d_in[3]; A.Wv = (const u16*)d_in[4]; A.Wo = (const u16*)d_in[5];
  A.w1 = (const u16*)d_in[8]; A.w2 = (const u16*)d_in[9]; A.w3 = (const u16*)d_in[10]; A.w4 = (const u16*)d_in[11];
  A.out = (u16*)d_out;
  char* ws = (char*)(((uintptr_t)d_ws + 2 * MiB - 1) & ~(uintptr_t)(2 * MiB - 1));
  A.ws = ws;
  hipMemsetAsync(ws, 0xFF, CTRL_BYTES, stream);
  hipFuncSetAttribute((const void*)k_mega, hipFuncAttributeMaxDynamicSharedMemorySize, LDS_BYTES);
  k_mega<<<256, 512, LDS_BYTES, stream>>>(A);
}
```

```cpp
#include <hip/hip_runtime.h>
#include <stdint.h>

typedef unsigned short u16;
typedef unsigned int u32;
typedef u32 u32x2 __attribute__((ext_vector_type(2)));
typedef u32 u32x4 __attribute__((ext_vector_type(4)));
typedef u32 u32x8 __attribute__((ext_vector_type(8)));
typedef float f32x2 __attribute__((ext_vector_type(2)));
typedef float f32x4 __attribute__((ext_vector_type(4)));
typedef short s16x4 __attribute__((ext_vector_type(4)));
typedef __bf16 bf16x2 __attribute__((ext_vector_type(2)));
typedef __bf16 bf16x8 __attribute__((ext_vector_type(8)));
typedef __bf16 bf16x16 __attribute__((ext_vector_type(16)));
typedef __attribute__((address_space(3))) s16x4* lds_s16x4_p;
#define DI __device__ __forceinline__

constexpr u32 MiB = 1u << 20;
constexpr u32 OFF_COMM = 0;
constexpr u32 OFF_FLAG = 768 * 1024;
constexpr u32 OFF_KX = 2 * MiB;
constexpr u32 OFF_QX = 18 * MiB;
constexpr u32 OFF_U  = 34 * MiB;
constexpr u32 OFF_BF = 98 * MiB;
constexpr u32 OFF_YP = 114 * MiB;
constexpr u32 OFF_XN = 130 * MiB;
constexpr u32 OFF_PT = 132 * MiB;
constexpr u32 OFF_PM = 136 * MiB;
constexpr u32 OFF_H  = 137 * MiB;
constexpr u32 OFF_YB = 139 * MiB;
constexpr u32 OFF_H1 = 141 * MiB;
constexpr u32 OFF_X1 = 143 * MiB;
constexpr u32 OFF_F  = 145 * MiB;
constexpr u32 WS_USED = 152 * MiB;
constexpr u32 KBS = 2048 * 64;
constexpr u32 FS = 128;
constexpr u32 FL_SEL = OFF_FLAG, FL_XN = FL_SEL + FS, FL_FOLD = FL_XN + 64 * FS, FL_QK = FL_FOLD + 128 * FS, FL_VU = FL_QK + 32 * FS,
              FL_XP = FL_VU + 32 * FS, FL_YE = FL_XP + 32 * FS, FL_T2 = FL_YE + 64 * FS, FL_B1 = FL_T2 + 32 * FS, FL_B2A = FL_B1 + 8 * FS,
              FL_B2B = FL_B2A + 8 * FS, FL_END = FL_B2B + 32 * FS;
constexpr u32 CTRL_BYTES = FL_END;
constexpr int SC1 = 16;
constexpr int LDS_MAIN = 144 * 1024;
constexpr int LDS_BYTES = LDS_MAIN;

DI u32 pk2(float a, float b) { f32x2 f = {a, b}; return __builtin_bit_cast(u32, __builtin_convertvector(f, bf16x2)); }
DI float lo16(u32 u) { return __uint_as_float(u << 16); }
DI float hi16(u32 u) { return __uint_as_float(u & 0xFFFF0000u); }
DI void bar() { asm volatile("" ::: "memory"); __builtin_amdgcn_s_waitcnt(0xC07F); __builtin_amdgcn_s_barrier(); asm volatile("" ::: "memory"); }
DI void drain() { asm volatile("" ::: "memory"); __builtin_amdgcn_s_waitcnt(0x0F70); asm volatile("" ::: "memory"); }
DI u32 lo8(u32 x) { return __builtin_amdgcn_update_dpp(x, x, 0x128, 0xf, 0xC, false); }
DI u32 hi8(u32 x) { return __builtin_amdgcn_update_dpp(x, x, 0x128, 0xf, 0x3, false); }
DI u32 now32() { return (u32)__builtin_amdgcn_s_memtime(); }
DI void wait_for(u32 t0, int d) { while ((int)(now32() - t0) < d) __builtin_amdgcn_s_sleep(1); }
DI float max6_red(float a, float b, float c, float d, float e, float f) {
  float t, u;
  asm("v_max3_f32 %0, %2, %3, %4\n\tv_max3_f32 %1, %5, %6, %7\n\tv_max_f32 %0, %0, %1\n\ts_nop 1\n\t"
      "v_max_f32_dpp %0, %0, %0 row_ror:8 row_mask:0xf bank_mask:0xf\n\tv_mov_b32 %1, %0\n\ts_nop 1\n\t"
      "v_permlane16_swap_b32 %0, %1\n\tv_max_f32 %0, %0, %1\n\tv_mov_b32 %1, %0\n\ts_nop 1\n\t"
      "v_permlane32_swap_b32 %0, %1\n\tv_max_f32 %0, %0, %1"
      : "=&v"(t), "=&v"(u) : "v"(a), "v"(b), "v"(c), "v"(d), "v"(e), "v"(f));
  return t;
}
DI float max3_red(float a, float b, float c) {
  float t, u;
  asm("v_max3_f32 %0, %2, %3, %4\n\ts_nop 1\n\t"
      "v_max_f32_dpp %0, %0, %0 row_ror:8 row_mask:0xf bank_mask:0xf\n\tv_mov_b32 %1, %0\n\ts_nop 1\n\t"
      "v_permlane16_swap_b32 %0, %1\n\tv_max_f32 %0, %0, %1\n\tv_mov_b32 %1, %0\n\ts_nop 1\n\t"
      "v_permlane32_swap_b32 %0, %1\n\tv_max_f32 %0, %0, %1"
      : "=&v"(t), "=&v"(u) : "v"(a), "v"(b), "v"(c));
  return t;
}
#define SB() __builtin_amdgcn_sched_barrier(0)
DI void hold(u32 t0, int ticks) { while ((int)((u32)__builtin_amdgcn_s_memrealtime() - t0) < ticks) __builtin_amdgcn_s_sleep(32); }
DI float xor8f(float x) { return __uint_as_float(__builtin_amdgcn_update_dpp(0u, __float_as_uint(x), 0x128, 0xf, 0xf, false)); }
DI float xor1f(float x) { return __uint_as_float(__builtin_amdgcn_update_dpp(0u, __float_as_uint(x), 0xB1, 0xf, 0xf, false)); }
DI float xor2f(float x) { return __uint_as_float(__builtin_amdgcn_update_dpp(0u, __float_as_uint(x), 0x4E, 0xf, 0xf, false)); }
DI float max1632(float x) {
  auto s = __builtin_amdgcn_permlane16_swap(__float_as_uint(x), __float_as_uint(x), false, false);
  x = fmaxf(__uint_as_float(s[0]), __uint_as_float(s[1]));
  auto t = __builtin_amdgcn_permlane32_swap(__float_as_uint(x), __float_as_uint(x), false, false);
  return fmaxf(__uint_as_float(t[0]), __uint_as_float(t[1]));
}
DI float sum1632(float x) {
  auto s = __builtin_amdgcn_permlane16_swap(__float_as_uint(x), __float_as_uint(x), false, false);
  x = __uint_as_float(s[0]) + __uint_as_float(s[1]);
  auto t = __builtin_amdgcn_permlane32_swap(__float_as_uint(x), __float_as_uint(x), false, false);
  return __uint_as_float(t[0]) + __uint_as_float(t[1]);
}
DI float red_max4(float x) { return max1632(fmaxf(x, xor8f(x))); }
DI float red_sum4(float x) { return sum1632(x + xor8f(x)); }
DI float red_sumq(float x) { x += xor1f(x); x += xor2f(x); return sum1632(x); }
DI f32x4 smf(u32x4 a, u32x8 b, f32x4 c, int idx) {
  return __builtin_amdgcn_smfmac_f32_16x16x64_bf16(__builtin_bit_cast(bf16x8, a), __builtin_bit_cast(bf16x16, b), c, idx, 0, 0);
}
DI f32x4 mma(u32x4 a, u32x4 b, f32x4 c) {
  return __builtin_amdgcn_mfma_f32_16x16x32_bf16(__builtin_bit_cast(bf16x8, a), __builtin_bit_cast(bf16x8, b), c, 0, 0, 0);
}
DI float gelu_t(float x) {
  float u = x * fmaf(x * x, 0.044715f * 0.7978845608028654f, 0.7978845608028654f);
  float e = __builtin_amdgcn_exp2f(u * (-2.f * 1.4426950408889634f));
  return x * __builtin_amdgcn_rcpf(1.f + e);
}
typedef __amdgpu_buffer_rsrc_t rsrc_t;
DI u32x4 ld16(rsrc_t r, u32 off) { return __builtin_amdgcn_raw_buffer_load_b128(r, off, 0, SC1); }
DI u32x2 ld8(rsrc_t r, u32 off) { return __builtin_amdgcn_raw_buffer_load_b64(r, off, 0, SC1); }
DI u32 ld4(rsrc_t r, u32 off) { return __builtin_amdgcn_raw_buffer_load_b32(r, off, 0, SC1); }
DI void st16(rsrc_t r, u32 off, u32x4 v) { __builtin_amdgcn_raw_buffer_store_b128(v, r, off, 0, SC1); }
DI void st8(rsrc_t r, u32 off, u32x2 v) { __builtin_amdgcn_raw_buffer_store_b64(v, r, off, 0, SC1); }
DI void st4(rsrc_t r, u32 off, u32 v) { __builtin_amdgcn_raw_buffer_store_b32(v, r, off, 0, SC1); }
constexpr int SPIN_MAX = 1 << 18;
constexpr u32 SENT = 0xFFFFFFFFu;
#define TLV 56
constexpr int TL0 = TLV;
DI u32 nz(u32 v) { return min(v, 0xFFFFFFFEu); }
DI u32x4 nz4(u32x4 v) { return u32x4{nz(v.x), nz(v.y), nz(v.z), nz(v.w)}; }
DI u32 mx4(const u32x4& m) { return max(max(m.x, m.y), max(m.z, m.w)); }

struct Args {
  const u16* x; const u16* mem0; const u16* Wq; const u16* Wk; const u16* Wv; const u16* Wo;
  const u16* w1; const u16* w2; const u16* w3; const u16* w4; u16* out; char* ws;
};

DI void wait_until(unsigned long long t0, int d) {
  while ((long long)(__builtin_amdgcn_s_memtime() - t0) < (long long)d) __builtin_amdgcn_s_sleep(1);
}
DI void wait_flags(rsrc_t rs, u32 off, int n, int need, int l, int& lim, int slp = 0) {
  for (int it = 0; it < lim; ++it) {
    int v = (int)ld4(rs, off + (l < n ? l : 0) * FS);
    if (__all(v >= need)) return;
    __builtin_amdgcn_s_sleep(2);
    for (int z = 0; z < slp; ++z) __builtin_amdgcn_s_sleep(16);
  }
  lim = 1;
}
DI int wave_min(int v) {
  v = min(v, (int)__builtin_amdgcn_update_dpp(0u, (u32)v, 0xB1, 0xf, 0xf, false));
  v = min(v, (int)__builtin_amdgcn_update_dpp(0u, (u32)v, 0x4E, 0xf, 0xf, false));
  v = min(v, (int)__builtin_amdgcn_update_dpp(0u, (u32)v, 0x141, 0xf, 0xf, false));
  v = min(v, (int)__builtin_amdgcn_update_dpp(0u, (u32)v, 0x140, 0xf, 0xf, false));
  auto s = __builtin_amdgcn_permlane16_swap((u32)v, (u32)v, false, false);
  v = min((int)s[0], (int)s[1]);
  auto t = __builtin_amdgcn_permlane32_swap((u32)v, (u32)v, false, false);
  v = min((int)t[0], (int)t[1]);
  return __builtin_amdgcn_readfirstlane(v);
}
DI void wait_flags_bk(rsrc_t rs, u32 off, int n, int need, int l, int& lim) {
  for (int it = 0; it < lim; ++it) {
    int v = (int)ld4(rs, off + (l < n ? l : 0) * FS);
    if (__all(v >= need)) return;
    int d = need - wave_min(v);
    __builtin_amdgcn_s_sleep(4);
    for (int z = 1; z < min(d, 8); ++z) __builtin_amdgcn_s_sleep(48);
  }
  lim = 1;
}
DI void wait_prog(rsrc_t rs, u32 off, int n, int need, int l, int& cache, int& lim) {
  if (cache >= need) return;
  for (int it = 0; it < lim; ++it) {
    int v = (int)ld4(rs, off + (l < n ? l : 0) * FS);
    if (__all(v >= need)) { cache = wave_min(v); return; }
    __builtin_amdgcn_s_sleep(2);
  }
  lim = 1;
}
DI u32x4 poll_mem(rsrc_t rs, u32 off, int& lim) {
  u32x4 m = {0xFFFFFFFFu, 0, 0, 0};
  bool ok = false;
  int it = 0;
  for (; it < lim; ++it) {
    if (!ok) m = ld16(rs, off);
    u32 mx = max(max(m.x, m.y), max(m.z, m.w));
    ok = mx != 0xFFFFFFFFu;
    if (__all(ok)) break;
    asm volatile("" ::: "memory");
  }
  if (it >= lim) lim = 1;
  return m;
}

DI bool mem_ok(const u32x4& m) { return __all(max(max(m.x, m.y), max(m.z, m.w)) != 0xFFFFFFFFu); }
#define MEMF() asm volatile("" ::: "memory")
template <int GAP>
DI void poll_mem3(rsrc_t rs, u32 off, u32 ldsAddr, int& lim, u32x4& pa, u32x4& pb, u32x4& pc, int& itc) {
  u32 t; int it;
#define PLD(R) "buffer_load_dwordx4 " R ", %[off], %[rs], 0 offen sc1\n\t"
#define PCK(x, y, z, w_, L) "s_waitcnt vmcnt(2)\n\tv_max_u32 %[t], " z ", " w_ "\n\tv_max3_u32 %[t], " x ", " y ", %[t]\n\tv_cmp_ne_u32 vcc, -1, %[t]\n\ts_cmp_eq_u64 vcc, exec\n\ts_cbranch_scc1 " L "\n\t"
  asm volatile(
      PLD("v[240:243]") "s_sleep %[gap]\n\t" PLD("v[244:247]") "s_sleep %[gap]\n\t" PLD("v[248:251]")
      "s_mov_b32 %[it], 0\n"
      "1:\n\t"
      PCK("v240", "v241", "v242", "v243", "2f") PLD("v[240:243]")
      PCK("v244", "v245", "v246", "v247", "3f") PLD("v[244:247]")
      PCK("v248", "v249", "v250", "v251", "4f") PLD("v[248:251]")
      "s_add_i32 %[it], %[it], 1\n\ts_cmp_lt_i32 %[it], %[lim]\n\ts_cbranch_scc1 1b\n\t"
      "s_waitcnt vmcnt(0)\n"
      "4:\n\tds_write_b128 %[lds], v[248:251]\n\ts_branch 5f\n"
      "3:\n\tds_write_b128 %[lds], v[244:247]\n\ts_branch 5f\n"
      "2:\n\tds_write_b128 %[lds], v[240:243]\n"
      "5:"
      : "={v[240:243]}"(pa), "={v[244:247]}"(pb), "={v[248:251]}"(pc), [t] "=&v"(t), [it] "=&s"(it)
      : [off] "v"(off), [rs] "s"(rs), [lim] "s"(lim), [lds] "v"(ldsAddr), [gap] "n"(GAP)
      : "vcc", "scc", "memory");
#undef PLD
#undef PCK
  if (it >= lim) lim = 1;
  itc = it;
}
template <int N>
DI void poll_retire(u32x4& pa, u32x4& pb, u32x4& pc) {
  asm volatile("s_waitcnt vmcnt(%3)" :: "{v[240:243]}"(pa), "{v[244:247]}"(pb), "{v[248:251]}"(pc), "n"(N));
}

template <bool KPERM>
DI u32x4 frag_tr(const char* L, int stride, int row0, int cb, int l) {
  const int g = l >> 4, q = (l & 15) >> 2;
  const int ra = row0 + (KPERM ? 4 * g : 8 * g) + q, rb = ra + (KPERM ? 16 : 4);
  u32x2 a = __builtin_bit_cast(u32x2, __builtin_amdgcn_ds_read_tr16_b64_v4i16((lds_s16x4_p)(L + ra * stride + cb)));
  u32x2 b = __builtin_bit_cast(u32x2, __builtin_amdgcn_ds_read_tr16_b64_v4i16((lds_s16x4_p)(L + rb * stride + cb)));
  return u32x4{a.x, a.y, b.x, b.y};
}
template <int PPR, int NP>
DI void slab_load(const u16* W, int ld, int t, u32x4 (&tmp)[NP]) {
#pragma unroll
  for (int i = 0; i < NP; ++i) { int id = i * 512 + t; tmp[i] = *(const u32x4*)(W + (size_t)(id / PPR) * ld + (id % PPR) * 8); }
}
template <int PPR, int NP>
DI void slab_store(char* L, int t, const u32x4 (&tmp)[NP]) {
#pragma unroll
  for (int i = 0; i < NP; ++i) { int id = i * 512 + t; *(u32x4*)(L + (id / PPR) * (PPR * 16 + 16) + (id % PPR) * 16) = tmp[i]; }
}

DI void ln_center(const u32x4 (&v)[4], f32x2 (&c)[16], float& var) {
  f32x2 s2 = {0.f, 0.f};
#pragma unroll
  for (int i = 0; i < 4; ++i)
#pragma unroll
    for (int k = 0; k < 4; ++k) { c[i * 4 + k] = f32x2{lo16(v[i][k]), hi16(v[i][k])}; s2 += c[i * 4 + k]; }
  float mean = red_sumq(s2.x + s2.y) * (1.f / 512.f);
  f32x2 m2 = {mean, mean}, q2 = {0.f, 0.f};
#pragma unroll
  for (int i = 0; i < 16; ++i) { c[i] -= m2; q2 += c[i] * c[i]; }
  var = red_sumq(q2.x + q2.y) * (1.f / 512.f);
}
DI u32x4 ln_pack(const f32x2 (&c)[16], int i, float r) {
  f32x2 r2 = {r, r};
  u32x4 o;
#pragma unroll
  for (int k = 0; k < 4; ++k) { f32x2 z = c[i * 4 + k] * r2; o[k] = pk2(z.x, z.y); }
  return o;
}

template <class F>
DI void project(const u32x8 (&wf)[2][8], const char* memL, int l, int idx, float (&v)[4], F&& extra) {
  f32x4 a0 = {0, 0, 0, 0}, a1 = {0, 0, 0, 0};
  u32x4 fr[8];
#pragma unroll
  for (int i = 0; i < 8; ++i) fr[i] = *(const u32x4*)(memL + (i << 10) + l * 16);
  __builtin_amdgcn_sched_barrier(0);
  extra();
  __builtin_amdgcn_sched_barrier(0);
#pragma unroll
  for (int i = 0; i < 8; ++i) { a0 = smf(fr[i], wf[0][i], a0, idx); a1 = smf(fr[i], wf[1][i], a1, idx); }
#pragma unroll
  for (int r = 0; r < 4; ++r) {
    auto s = __builtin_amdgcn_permlane32_swap(__float_as_uint(a0[r]), __float_as_uint(a1[r]), false, false);
    v[r] = __uint_as_float(s[0]) + __uint_as_float(s[1]);
  }
}

#define XGAP 3
#define SLPV 0
#define BD0 1000
#define BD1 1200
#define MODEV 0
#define XD 0
#define DYV 1800
#define DYMV 800
#define YREP 4
#define PRM(j) (j)
#define PRMP(j) (j)
#define PLAY 0
#define ADEC 16
#define AINC 128

DI void role_X(const Args& A, char* smem, rsrc_t rs, int h, int rep, u32 t0) {
  const int t = threadIdx.x, l = t & 63, w = __builtin_amdgcn_readfirstlane(t >> 6);
  const int g = l >> 4;
  int lim = SPIN_MAX;
  u32x8 wf[2][8];
  hold(t0, XD);
  {
    char* st = smem + w * (128 * 80);
    const u16* W = (w < 4 ? A.Wq : A.Wk) + h * 128 + (w & 3) * 32;
#pragma unroll
    for (int c = 0; c < 4; ++c) {
      u32x4 tmp[8];
#pragma unroll
      for (int i = 0; i < 8; ++i) {
        int row = (l >> 2) + 16 * i;
        int kc = 2 * c + (row >> 6);
        tmp[i] = *(const u32x4*)(W + (size_t)(kc * 64 + (row & 63)) * 4096 + (l & 3) * 8);
      }
#pragma unroll
      for (int i = 0; i < 8; ++i) *(u32x4*)(st + ((l >> 2) + 16 * i) * 80 + (l & 3) * 16) = tmp[i];
      asm volatile("s_waitcnt lgkmcnt(0)" ::: "memory");
      const int q = (l & 15) >> 2, p = l & 3;
      const int rbase = 16 * (g >> 1) + 4 * (g & 1) + (q & 1) + 8 * (q >> 1);
#pragma unroll
      for (int sl = 0; sl < 2; ++sl)
#pragma unroll
        for (int tt = 0; tt < 2; ++tt)
#pragma unroll
          for (int u = 0; u < 4; ++u) {
            int row = sl * 64 + rbase + 32 * (u >> 1) + 2 * (u & 1);
            s16x4 r = __builtin_amdgcn_ds_read_tr16_b64_v4i16((lds_s16x4_p)(st + row * 80 + (tt * 16 + p * 4) * 2));
            u32x2 r2 = __builtin_bit_cast(u32x2, r);
            wf[tt][2 * c + sl][2 * u] = r2.x; wf[tt][2 * c + sl][2 * u + 1] = r2.y;
          }
      asm volatile("s_waitcnt lgkmcnt(0)" ::: "memory");
    }
  }
  wait_flags(rs, FL_SEL, 1, 0, l, lim);
  u32 sel = __builtin_amdgcn_readfirstlane(ld4(rs, FL_SEL)) & 1;
  if (MODEV & 1) sel = 0;
  if (MODEV & 2) sel = 1;
  __syncthreads();
  char* memL = smem;
  char* qkL = smem + 8192;
  const int idx = (l & 8) ? 0xEEEE : 0x4444;
  const u32 commB = OFF_COMM + sel * 8192;
  const u32 memOff = commB + PRM(rep) * 16384 + w * 1024 + l * 16;
  const u32 rst = PLAY ? 16384 : 65536;
  const u32 pOff = commB + (32 + (h >> 3) * (PLAY ? 4 : 1)) * 16384;
  const int a = (l >> 3) & 1, r8 = l & 7;
  const u32 pTok = pOff + (h & 7) * 512 + (g * 8 + r8) * 16 + a * rst;
  const u32 pMM = pOff + 4096 + (h & 7) * 128 + r8 * 16 + ((g & 1) * 2 + a) * rst;
  const float csc = 0.08838834764831845f * 1.4426950408889634f;
  const bool isT1 = (w == 1 || w == 2);
  const int tt1 = w - 1;
  char* KQL = smem + 16384;
  const u32 kqOff = (w < 6 ? OFF_KX : OFF_QX) + (u32)h * 8192 + ((w & 1) * 16 + (l & 15)) * 256 + g * 16;
  int qkdone = -1;
  u32x4 aux[8];
#define kq aux
#pragma unroll
  for (int i = 0; i < 8; ++i) aux[i] = u32x4{0, 0, 0, 0};
  if (w >= 4) {
    wait_prog(rs, FL_QK + h * FS, 1, 0, l, qkdone, lim);
#pragma unroll
    for (int i = 0; i < 4; ++i) kq[i] = ld16(rs, kqOff + i * 64);
  }
  const u32 ptOff = OFF_PT + (u32)(h * 2 + (tt1 & 1)) * 1024 + l * 16;
  const u32 pmOff = OFF_PM + (u32)((h >> 2) * 2 + (tt1 & 1)) * 1024 + (h & 3) * 256 + (l & 15) * 16 + (g & 1) * 8;
  if (isT1) {
#pragma unroll
    for (u32 u = TL0; u < 64; ++u) {
      st16(rs, ptOff + u * 65536, u32x4{SENT, SENT, SENT, SENT});
      if (g < 2) st8(rs, pmOff + u * 16384, u32x2{SENT, SENT});
    }
  }
  if (w >= 4) {
#pragma unroll
    for (int i = 0; i < 4; ++i) *(u32x4*)(KQL + ((w - 4) * 4 + i) * 1024 + l * 16) = kq[i];
  }
  bar();
  if (w == 0) {
#pragma unroll
    for (int tt = 0; tt < 2; ++tt)
#pragma unroll
      for (int ks = 0; ks < 4; ++ks) aux[tt * 4 + ks] = *(const u32x4*)(KQL + (tt * 4 + ks) * 1024 + l * 16);
  }
  if (w >= 4) wait_prog(rs, FL_QK + h * FS, 1, 1, l, qkdone, lim);
  u32x4 m = *(const u32x4*)(A.mem0 + (l & 7) * 512 + w * 64 + (l >> 3) * 8);
  u32x4 pa = m, pb = m, pc = m;
  int itc = 0;
  *(u32x4*)(memL + w * 1024 + l * 16) = m;
  const u32 memLds = (u32)(size_t)(__attribute__((address_space(3))) char*)(memL + w * 1024 + l * 16);
  for (int s = 0; s < 64; ++s) {
    char* kqb = KQL + (s & 1) * 16384;
    bar();
    float v[4];
    project(wf, memL, l, idx, v, [&]() {
      if (w >= 4 && s < 63 && !(MODEV & 32)) {
#pragma unroll
        for (int i = 0; i < 4; ++i) kq[i] = ld16(rs, kqOff + (u32)(s + 1) * (32 * 8192) + i * 64);
      }
    });
    {
      *(u32x2*)(qkL + (w * 32 + (g >> 1) * 16 + (l & 15)) * 16 + (g & 1) * 8) = u32x2{pk2(v[0], v[1]), pk2(v[2], v[3])};
    }
    if (isT1) drain();
    bar();
    if (w >= 4 && s < 63 && !(MODEV & 32)) poll_retire<4>(pa, pb, pc); else poll_retire<0>(pa, pb, pc);
    if (w == 3 && s >= 1 && l == 0) st4(rs, FL_XP + h * FS, (u32)(s - 1));
    if (w == 0 && s < 63) {
      u32x4 qf[4], mf[4];
#pragma unroll
      for (int ks = 0; ks < 4; ++ks) { qf[ks] = frag_tr<false>(qkL, 16, ks * 32, (l & 1) * 8, l); SB(); }
#pragma unroll
      for (int ks = 0; ks < 4; ++ks) { mf[ks] = frag_tr<false>(qkL + 2048, 16, ks * 32, (l & 1) * 8, l); SB(); }
      float ci = g >= 2 ? -1e30f : 0.f;
      f32x4 d0 = {ci, ci, ci, ci}, d1 = {0, 0, 0, 0}, d2 = {0, 0, 0, 0};
#pragma unroll
      for (int ks = 0; ks < 4; ++ks) { d1 = mma(aux[ks], qf[ks], d1); d2 = mma(aux[4 + ks], qf[ks], d2); SB(); }
      d0 = mma(mf[0], qf[0], d0); d0 = mma(mf[1], qf[1], d0); SB();
      float e0 = a ? d2[0] : d1[0], e1 = a ? d2[1] : d1[1], e2 = a ? d2[2] : d1[2], e3 = a ? d2[3] : d1[3];
      float m4 = fmaxf(fmaxf(e0, e1), fmaxf(e2, e3));
      SB();
      d0 = mma(mf[2], qf[2], d0); d0 = mma(mf[3], qf[3], d0); SB();
      float e4 = a ? d0[2] : d0[0], e5 = a ? d0[3] : d0[1];
      float mx = max3_red(m4, e4, e5);
      float mc = -mx * csc;
      e0 = __builtin_amdgcn_exp2f(fmaf(e0, csc, mc)); e1 = __builtin_amdgcn_exp2f(fmaf(e1, csc, mc));
      e2 = __builtin_amdgcn_exp2f(fmaf(e2, csc, mc)); e3 = __builtin_amdgcn_exp2f(fmaf(e3, csc, mc));
      e4 = __builtin_amdgcn_exp2f(fmaf(e4, csc, mc)); e5 = __builtin_amdgcn_exp2f(fmaf(e5, csc, mc));
      float sum = ((e0 + e1) + (e2 + e3)) + (e4 + e5);
      sum = red_sum4(sum);
      float rl = __builtin_amdgcn_rcpf(sum);
      rl = (s & 1) ? -rl : rl;
      u32x2 pt = {pk2(e0 * rl, e1 * rl), pk2(e2 * rl, e3 * rl)};
      u32 pm = pk2(e4 * rl, e5 * rl);
      const u32x4 gt = {lo8(pt.x), lo8(pt.y), hi8(pt.x), hi8(pt.y)};
      st16(rs, pTok, gt); st16(rs, pTok + 2 * rst, gt);
      const u32 ml = lo8(pm), mh = hi8(pm);
      auto sl = __builtin_amdgcn_permlane16_swap(ml, ml, false, false);
      auto sh = __builtin_amdgcn_permlane16_swap(mh, mh, false, false);
      if (l < 32) st16(rs, pMM, u32x4{sl[0], sh[0], sl[1], sh[1]});
    }
    if (isT1 && !(MODEV & 8)) {
      __builtin_amdgcn_s_sleep(3);
      u32x4 mf[4], kt[2][4], qt[4];
#pragma unroll
      for (int ks = 0; ks < 4; ++ks) {
        qt[ks] = *(const u32x4*)(kqb + (8 + tt1 * 4 + ks) * 1024 + l * 16);
        kt[0][ks] = *(const u32x4*)(kqb + ks * 1024 + l * 16); kt[1][ks] = *(const u32x4*)(kqb + (4 + ks) * 1024 + l * 16);
        mf[ks] = frag_tr<false>(qkL + 2048, 16, ks * 32, (l & 1) * 8, l);
      }
      f32x4 d0 = {0, 0, 0, 0}, d1 = {0, 0, 0, 0}, d2 = {0, 0, 0, 0};
#pragma unroll
      for (int ks = 0; ks < 4; ++ks) { d1 = mma(kt[0][ks], qt[ks], d1); d2 = mma(kt[1][ks], qt[ks], d2); }
#pragma unroll
      for (int ks = 0; ks < 4; ++ks) d0 = mma(mf[ks], qt[ks], d0);
      const int qi = tt1 * 16 + (l & 15);
      float e[12];
#pragma unroll
      for (int r = 0; r < 4; ++r) {
        e[r] = (4 * g + r <= qi) ? d1[r] : -1e30f;
        e[4 + r] = (16 + 4 * g + r <= qi) ? d2[r] : -1e30f;
        e[8 + r] = (g < 2) ? d0[r] : -1e30f;
      }
      float mx = e[0];
#pragma unroll
      for (int r = 1; r < 12; ++r) mx = fmaxf(mx, e[r]);
      mx = max1632(mx);
      float mc = -mx * csc, sum = 0.f;
#pragma unroll
      for (int r = 0; r < 12; ++r) { e[r] = __builtin_amdgcn_exp2f(fmaf(e[r], csc, mc)); sum += e[r]; }
      sum = sum1632(sum);
      float rl = __builtin_amdgcn_rcpf(sum);
      st16(rs, ptOff + (u32)s * 65536, nz4(u32x4{pk2(e[0] * rl, e[1] * rl), pk2(e[2] * rl, e[3] * rl), pk2(e[4] * rl, e[5] * rl), pk2(e[6] * rl, e[7] * rl)}));
      if (g < 2) st8(rs, pmOff + (u32)s * 16384, u32x2{nz(pk2(e[8] * rl, e[9] * rl)), nz(pk2(e[10] * rl, e[11] * rl))});
    }
    if (s < 63) {
      char* kqn = KQL + ((s + 1) & 1) * 16384;
      if (w >= 4) {
        __builtin_amdgcn_s_sleep(4);
        wait_prog(rs, FL_QK + h * FS, 1, min(s + 2, 63), l, qkdone, lim);
#pragma unroll
        for (int i = 0; i < 4; ++i) *(u32x4*)(kqn + ((w - 4) * 4 + i) * 1024 + l * 16) = kq[i];
      }
      bar();
      if (w == 0) {
#pragma unroll
        for (int tt = 0; tt < 2; ++tt)
#pragma unroll
          for (int ks = 0; ks < 4; ++ks) aux[tt * 4 + ks] = *(const u32x4*)(kqn + (tt * 4 + ks) * 1024 + l * 16);
      }
      poll_mem3<XGAP>(rs, memOff + ((s + 1) & 3) * 131072, memLds, lim, pa, pb, pc, itc);
    }
  }
  drain();
  bar();
  if (t == 192) st4(rs, FL_XP + h * FS, 63u);
}

#undef kq
DI void fold_task(const Args& A, char* smem, rsrc_t rs, int task) {
  const int t = threadIdx.x, l = t & 63, w = __builtin_amdgcn_readfirstlane(t >> 6);
  const int g = l >> 4, h = task >> 2, q = task & 3;
  const int r = l & 15, gr = r >> 2, rg = r & 3;
  const int drow = 64 * w + 16 * (gr >> 1) + 4 * (gr & 1) + 8 * (rg >> 1) + (rg & 1);
  const u16* Wv = A.Wv + (size_t)drow * 4096 + h * 128 + g * 8;
  u32x4 tmp[4], af[4][4];
  slab_load<16, 4>(A.Wo + (size_t)h * 128 * 512 + 128 * q, 512, t, tmp);
#pragma unroll
  for (int u = 0; u < 4; ++u)
#pragma unroll
    for (int ks = 0; ks < 4; ++ks) af[u][ks] = *(const u32x4*)(Wv + (size_t)(32 * (u >> 1) + 2 * (u & 1)) * 4096 + ks * 32);
  __builtin_amdgcn_sched_barrier(0);
  slab_store<16, 4>(smem, t, tmp);
  __syncthreads();
#pragma unroll
  for (int ct = 0; ct < 8; ++ct) {
    u32x4 wo[4];
#pragma unroll
    for (int ks = 0; ks < 4; ++ks) wo[ks] = frag_tr<false>(smem, 272, ks * 32, (16 * ct + 4 * (l & 3)) * 2, l);
    const int c = 128 * q + 16 * ct + r, cc = c >> 3, jj = c & 7;
#pragma unroll
    for (int half = 0; half < 2; ++half) {
      f32x4 a0 = {0, 0, 0, 0}, a1 = {0, 0, 0, 0};
#pragma unroll
      for (int ks = 0; ks < 4; ++ks) { a0 = mma(af[2 * half][ks], wo[ks], a0); a1 = mma(af[2 * half + 1][ks], wo[ks], a1); }
      const u32 o = (u32)((((((cc * 8 + (h >> 2)) * 2 + ((h >> 1) & 1)) * 8 + w) * 2 + half) * 64 + g * 16 + (h & 1) * 8 + jj)) * 16;
      st16(rs, OFF_BF + o, u32x4{pk2(a0[0], a0[1]), pk2(a0[2], a0[3]), pk2(a1[0], a1[1]), pk2(a1[2], a1[3])});
    }
  }
  drain();
  __syncthreads();
  if (t == 0) st4(rs, FL_FOLD + task * FS, 1u);
}

DI void ln_task(const Args& A, rsrc_t rs, int s0) {
  const int t = threadIdx.x, l = t & 63, w = __builtin_amdgcn_readfirstlane(t >> 6);
  const int g = l & 3, tk = (l >> 2) & 3, kq = l >> 4, tok = 4 * w + tk;
  u32x4 v[2][4];
#pragma unroll
  for (int u = 0; u < 2; ++u)
#pragma unroll
    for (int i = 0; i < 4; ++i) v[u][i] = *(const u32x4*)(A.x + (size_t)(32 * (s0 + 32 * u) + tok) * 512 + 32 * (4 * i + kq) + 8 * g);
#pragma unroll
  for (int u = 0; u < 2; ++u) {
    f32x2 c[16]; float var;
    ln_center(v[u], c, var);
    float r = __builtin_amdgcn_rsqf(var + 1e-5f);
#pragma unroll
    for (int i = 0; i < 4; ++i)
      st16(rs, OFF_XN + (u32)(s0 + 32 * u) * 32768 + (u32)((((tok >> 4) * 16 + 4 * i + kq) * 64) + g * 16 + ((tok & 15) ^ (4 * g))) * 16, ln_pack(c, i, r));
  }
  drain();
  __syncthreads();
  if (t < 2) st4(rs, FL_XN + (s0 + 32 * t) * FS, 1u);
}

DI void role_Y(const Args& A, char* smem, rsrc_t rs, int cc, int rep) {
  const int t = threadIdx.x, l = t & 63, w = __builtin_amdgcn_readfirstlane(t >> 6);
  const int g = l >> 4;
  int lim = SPIN_MAX;
  for (int u = TL0 + w; u < 64; u += 8) {
#pragma unroll
    for (int i = 0; i < 4; ++i) {
      int id = i * 64 + l;
      st16(rs, OFF_YP + (u32)((u * 32 + (id >> 3)) * 512 + cc * 8 + (id & 7)) * 16, u32x4{SENT, SENT, SENT, SENT});
    }
  }
  fold_task(A, smem, rs, (((cc & 3) + 4 * (cc >> 4)) << 2) | ((cc >> 2) & 3));
  for (int it = 0; it < lim; ++it) {
    int v = (int)ld4(rs, FL_FOLD + (u32)((4 * w + (l & 3)) * 4 + (cc >> 4)) * FS);
    if (__all(v >= 0)) break;
    __builtin_amdgcn_s_sleep(2);
  }
  u32x8 wf[2][8];
#pragma unroll
  for (int tt = 0; tt < 2; ++tt)
#pragma unroll
    for (int i = 0; i < 8; ++i) {
      int kc = i;
      u32 o = OFF_BF + (u32)((((cc * 8 + w) * 2 + tt) * 8 + kc) * 2) * 1024 + l * 16;
      u32x4 lo = ld16(rs, o), hi = ld16(rs, o + 1024);
      wf[tt][i] = u32x8{lo.x, lo.y, lo.z, lo.w, hi.x, hi.y, hi.z, hi.w};
    }
  wait_flags(rs, FL_SEL, 1, 0, l, lim);
  u32 sel = __builtin_amdgcn_readfirstlane(ld4(rs, FL_SEL)) & 1;
  if (MODEV & 1) sel = 0;
  if (MODEV & 2) sel = 1;
  char* memL = smem;
  char* YL = smem + 8192;
  float* RL = (float*)(smem + 12288);
  const int idx = (l & 8) ? 0xEEEE : 0x4444;
  const u32 commB = OFF_COMM + sel * 8192;
  const u32 memOff = commB + (PRM(rep) + 4) * 16384 + w * 1024 + l * 16;
  const int set = (l >> 3) & 1, r8 = l & 7;
  const u32 pBase = commB + (32 + (PLAY ? (w & 3) * 4 + PRMP(rep) : rep * 4 + (w & 3))) * 16384;
  const u32 pTok = pBase + set * 512 + (g * 8 + r8) * 16;
  const u32 pMM = pBase + 4096 + (set * 4 + g) * 128 + r8 * 16;
  const u32 uOff = OFF_U + (u32)cc * 16384 + (u32)(8 * (w & 3) + set) * 512 + g * 128 + r8 * 16;
  char* UL = smem + 16384;
  int vudone = -1;
  u32x4 un[4];
  if (w >= 4) {
    wait_prog(rs, FL_VU, 32, 0, l, vudone, lim);
#pragma unroll
    for (int mm = 0; mm < 4; ++mm) un[mm] = ld16(rs, uOff + mm * 1024);
  }
  u32x4 m = *(const u32x4*)(A.mem0 + (l & 7) * 512 + w * 64 + (l >> 3) * 8);
  const int rlIdx = (l & 7) * 8 + ((l >> 4) & 1) + 2 * (l >> 5) + 4 * ((l >> 3) & 1);
  const u32 pubOff = commB + (l >> 3) * 16384 + cc * 128 + (l & 7) * 16;
  u32 tref = now32();
  int dP = DYV;
  for (int s = 0; s < 64; ++s) {
    *(u32x4*)(memL + w * 1024 + l * 16) = m;
    char* ub = UL + (s & 1) * 16384 + (w & 3) * 4096 + l * 16;
    if (w >= 4) {
#pragma unroll
      for (int mm = 0; mm < 4; ++mm) *(u32x4*)(ub + mm * 1024) = un[mm];
    }
    bar();
    if (w >= 4 && s < 62 && !(MODEV & 16)) {
      wait_prog(rs, FL_VU, 32, s + 1, l, vudone, lim);
#pragma unroll
      for (int mm = 0; mm < 4; ++mm) un[mm] = ld16(rs, uOff + (u32)(s + 1) * (64 * 16384) + mm * 1024);
    }
    if (w == 5 && s >= 2)
      st16(rs, commB + (((s - 1) & 3) * 8 + (l >> 3)) * 16384 + cc * 128 + (l & 7) * 16, u32x4{0xFFFFFFFFu, 0xFFFFFFFFu, 0xFFFFFFFFu, 0xFFFFFFFFu});
    float v[4];
    u32x4 uf[4];
    project(wf, memL, l, idx, v, [&]() {
      if (w < 4) {
#pragma unroll
        for (int mm = 0; mm < 4; ++mm) uf[mm] = *(const u32x4*)(ub + mm * 1024);
      }
    });
    {
      u32x2 pk = {pk2(v[0], v[1]), pk2(v[2], v[3])};
      int head = 4 * w + 2 * (g >> 1) + ((l >> 3) & 1);
      *(u32x2*)(YL + (head * 8 + (l & 7)) * 16 + (g & 1) * 8) = pk;
    }
    bar();
    if (w >= 5 && s < 61) wait_prog(rs, FL_VU, 32, s + 2, l, vudone, lim);
    if (w == 4 && !(MODEV & 128)) {
#pragma unroll
      for (int i = 0; i < 4; ++i) {
        int id = i * 64 + l;
        u32x4 y = *(const u32x4*)(YL + id * 16);
        st16(rs, OFF_YP + (u32)((s * 32 + (id >> 3)) * 512 + cc * 8 + (id & 7)) * 16, nz4(y));
      }
    }
    if (w == 4 && s < 61) wait_prog(rs, FL_VU, 32, s + 2, l, vudone, lim);
    if (s < 63) {
      if (w < 4) {
        u32x4 yf = *(const u32x4*)(YL + ((8 * w + 4 * set + g) * 8 + r8) * 16);
        u32x4 pf[5];
        wait_for(tref, dP);
        const u32 tm = (s & 1) ? 0x80008000u : 0u;
        bool ok0 = false, ok1 = false, ok2 = false, ok3 = false, ok4 = false;
        int it = 0;
        for (; it < lim; ++it) {
          if (!ok0) pf[0] = ld16(rs, pTok);
          if (!ok1) pf[1] = ld16(rs, pTok + 1024);
          if (!ok2) pf[2] = ld16(rs, pTok + 2048);
          if (!ok3) pf[3] = ld16(rs, pTok + 3072);
          if (!ok4) pf[4] = ld16(rs, pMM);
          ok0 = (((pf[0].x ^ tm) | (pf[0].z ^ tm)) & 0x80008000u) == 0;
          ok1 = (((pf[1].x ^ tm) | (pf[1].z ^ tm)) & 0x80008000u) == 0;
          ok2 = (((pf[2].x ^ tm) | (pf[2].z ^ tm)) & 0x80008000u) == 0;
          ok3 = (((pf[3].x ^ tm) | (pf[3].z ^ tm)) & 0x80008000u) == 0;
          ok4 = (((pf[4].x ^ tm) | (pf[4].y ^ tm) | (pf[4].z ^ tm) | (pf[4].w ^ tm)) & 0x80008000u) == 0;
          if (__all(ok0 && ok1 && ok2 && ok3 && ok4)) break;
          asm volatile("" ::: "memory");
        }
        if (it >= lim) lim = 1;
        dP += (it == 0) ? -ADEC : min(it, 4) * AINC;
        dP = max(min(dP, 6000), 0);
        f32x4 d = {0, 0, 0, 0}, d2 = {0, 0, 0, 0};
        d = mma(pf[0], uf[0], d); d2 = mma(pf[1], uf[1], d2); d = mma(pf[2], uf[2], d); d2 = mma(pf[3], uf[3], d2); d = mma(pf[4], yf, d);
        d += d2;
        if ((g >> 1) == set) {
          float* dst = RL + (w * 2 + set) * 64 + (4 * (g & 1)) * 8 + r8;
          dst[0] = d[0]; dst[8] = d[1]; dst[16] = d[2]; dst[24] = d[3];
        }
      }
      if (w == 4) drain();
      bar();
      if (w == 0) {
        const float* R = RL + rlIdx;
        float x0 = R[0], x1 = R[64], x2 = R[128], x3 = R[192], x4 = R[256], x5 = R[320], x6 = R[384], x7 = R[448];
        float sum = ((x0 + x1) + (x2 + x3)) + ((x4 + x5) + (x6 + x7));
        auto s16 = __builtin_amdgcn_permlane16_swap(__float_as_uint(sum), __float_as_uint(sum), false, false);
        u32 pk = pk2(__uint_as_float(s16[0]), __uint_as_float(s16[1]));
        if (s & 1) pk ^= 0x80008000u;
        pk = min(pk, 0xFFFFFFFEu);
        auto s32 = __builtin_amdgcn_permlane32_swap(pk, pk, false, false);
        u32x4 o = {lo8(s32[0]), lo8(s32[1]), hi8(s32[0]), hi8(s32[1])};
        if (l < 32 || YREP) st16(rs, pubOff + ((s + 1) & 3) * 131072, o);
      }
      const u32 tpub = now32();
      wait_for(tpub, DYMV);
      m = poll_mem(rs, memOff + ((s + 1) & 3) * 131072, lim);
      tref = now32();
      drain();
    }
  }
}

DI void role_QK(const Args& A, char* smem, rsrc_t rs, int h) {
  const int t = threadIdx.x, l = t & 63, w = __builtin_amdgcn_readfirstlane(t >> 6);
  const int g = l >> 4;
  int lim = SPIN_MAX;
  u32x4 wf[2][16];
  {
    u32x4 tmp[2][16];
#pragma unroll
    for (int rnd = 0; rnd < 2; ++rnd)
#pragma unroll
      for (int i = 0; i < 16; ++i) {
        int id = i * 512 + t, row = id >> 5, c8 = id & 31;
        tmp[rnd][i] = *(const u32x4*)((c8 < 16 ? A.Wq : A.Wk) + (size_t)(rnd * 256 + row) * 4096 + h * 128 + (c8 & 15) * 8);
      }
    SB();
#pragma unroll
    for (int rnd = 0; rnd < 2; ++rnd) {
      slab_store<32, 16>(smem, t, tmp[rnd]);
      __syncthreads();
#pragma unroll
      for (int ks = 0; ks < 8; ++ks)
#pragma unroll
        for (int te = 0; te < 2; ++te) wf[te][rnd * 8 + ks] = frag_tr<false>(smem, 528, ks * 32, (32 * w + 8 * (l & 3) + 4 * te) * 2, l);
      __syncthreads();
    }
  }
  if (w == 0) wait_flags(rs, FL_XN, 8, 0, l, lim);
  __syncthreads();
  u32x4 pf[4];
#pragma unroll
  for (int i = 0; i < 4; ++i) pf[i] = ld16(rs, OFF_XN + (i * 512 + t) * 16);
#pragma unroll
  for (int i = 0; i < 4; ++i) *(u32x4*)(smem + (i * 512 + t) * 16) = pf[i];
  bar();
  const int slot = (g * 16 + ((l & 15) ^ (4 * g))) * 16;
  const u32 oOff = (w < 4 ? OFF_QX : OFF_KX) + (u32)h * 8192 + (l & 15) * 256 + ((w & 3) * 32 + 8 * g) * 2;
  for (int s = 0; s < 64; ++s) {
    if (s == 6) { if (w == 0) wait_flags(rs, FL_XN, 64, 0, l, lim); bar(); }
#pragma unroll
    for (int i = 0; i < 4; ++i) pf[i] = ld16(rs, OFF_XN + (u32)min(s + 1, 63) * 32768 + (i * 512 + t) * 16);
    __builtin_amdgcn_sched_barrier(0);
    const char* xb = smem + (s & 1) * 32768 + slot;
    f32x4 acc[2][2] = {};
    u32x4 bb[2][4][2];
#pragma unroll
    for (int k = 0; k < 4; ++k) { bb[0][k][0] = *(const u32x4*)(xb + k * 1024); bb[0][k][1] = *(const u32x4*)(xb + 16384 + k * 1024); }
#pragma unroll
    for (int gq = 0; gq < 4; ++gq) {
      if (gq < 3) {
#pragma unroll
        for (int k = 0; k < 4; ++k) {
          bb[(gq + 1) & 1][k][0] = *(const u32x4*)(xb + ((gq + 1) * 4 + k) * 1024);
          bb[(gq + 1) & 1][k][1] = *(const u32x4*)(xb + 16384 + ((gq + 1) * 4 + k) * 1024);
        }
      }
      __builtin_amdgcn_sched_barrier(0);
#pragma unroll
      for (int k = 0; k < 4; ++k) {
        const int ks = gq * 4 + k;
        acc[0][0] = mma(wf[0][ks], bb[gq & 1][k][0], acc[0][0]); acc[1][0] = mma(wf[1][ks], bb[gq & 1][k][0], acc[1][0]);
        acc[0][1] = mma(wf[0][ks], bb[gq & 1][k][1], acc[0][1]); acc[1][1] = mma(wf[1][ks], bb[gq & 1][k][1], acc[1][1]);
      }
      __builtin_amdgcn_sched_barrier(0);
    }
    drain();
#pragma unroll
    for (int i = 0; i < 4; ++i) *(u32x4*)(smem + ((s + 1) & 1) * 32768 + (i * 512 + t) * 16) = pf[i];
#pragma unroll
    for (int tk = 0; tk < 2; ++tk)
      st16(rs, oOff + (u32)s * (32 * 8192) + tk * 4096,
           u32x4{pk2(acc[0][tk][0], acc[0][tk][1]), pk2(acc[0][tk][2], acc[0][tk][3]), pk2(acc[1][tk][0], acc[1][tk][1]), pk2(acc[1][tk][2], acc[1][tk][3])});
    if (s <= 2 || s == 63) drain();
    bar();
    if (t == 0) st4(rs, FL_QK + h * FS, (u32)((s <= 2 || s == 63) ? s : s - 1));
  }
}

DI void role_VU(const Args& A, char* smem, rsrc_t rs, int h) {
  const int t = threadIdx.x, l = t & 63, w = __builtin_amdgcn_readfirstlane(t >> 6);
  const int g = l >> 4;
  int lim = SPIN_MAX;
  u32x4 wv[16], wo[4][4];
  {
    u32x4 tmp[16];
    slab_load<16, 16>(A.Wv + h * 128, 4096, t, tmp);
    slab_store<16, 16>(smem, t, tmp);
    __syncthreads();
    slab_load<64, 16>(A.Wo + (size_t)h * 128 * 512, 512, t, tmp);
#pragma unroll
    for (int ks = 0; ks < 16; ++ks) wv[ks] = frag_tr<false>(smem, 272, ks * 32, (16 * w + 4 * (l & 3)) * 2, l);
    __syncthreads();
    slab_store<64, 16>(smem, t, tmp);
    __syncthreads();
#pragma unroll
    for (int ct = 0; ct < 4; ++ct)
#pragma unroll
      for (int ks = 0; ks < 4; ++ks) wo[ct][ks] = frag_tr<true>(smem, 1040, ks * 32, (64 * w + 16 * ct + 4 * (l & 3)) * 2, l);
    __syncthreads();
  }
  if (w == 0) wait_flags(rs, FL_XN, 8, 0, l, lim);
  __syncthreads();
  u32x4 pf[4];
#pragma unroll
  for (int i = 0; i < 4; ++i) pf[i] = ld16(rs, OFF_XN + (i * 512 + t) * 16);
#pragma unroll
  for (int i = 0; i < 4; ++i) *(u32x4*)(smem + (i * 512 + t) * 16) = pf[i];
  bar();
  char* VL = smem + 32768;
  const int slot = (g * 16 + ((l & 15) ^ (4 * g))) * 16;
  const u32 uOff = OFF_U + (u32)(8 * w + ((l >> 3) & 1)) * 16384 + (u32)h * 512 + g * 128 + (l & 7) * 16;
  for (int s = 0; s < 64; ++s) {
    if (s == 6) { if (w == 0) wait_flags(rs, FL_XN, 64, 0, l, lim); bar(); }
#pragma unroll
    for (int i = 0; i < 4; ++i) pf[i] = ld16(rs, OFF_XN + (u32)min(s + 1, 63) * 32768 + (i * 512 + t) * 16);
    __builtin_amdgcn_sched_barrier(0);
    const char* xb = smem + slot;
    f32x4 av[2] = {};
    u32x4 bb[2][4][2];
#pragma unroll
    for (int k = 0; k < 4; ++k) { bb[0][k][0] = *(const u32x4*)(xb + k * 1024); bb[0][k][1] = *(const u32x4*)(xb + 16384 + k * 1024); }
#pragma unroll
    for (int gq = 0; gq < 4; ++gq) {
      if (gq < 3) {
#pragma unroll
        for (int k = 0; k < 4; ++k) {
          bb[(gq + 1) & 1][k][0] = *(const u32x4*)(xb + ((gq + 1) * 4 + k) * 1024);
          bb[(gq + 1) & 1][k][1] = *(const u32x4*)(xb + 16384 + ((gq + 1) * 4 + k) * 1024);
        }
      }
      __builtin_amdgcn_sched_barrier(0);
#pragma unroll
      for (int k = 0; k < 4; ++k) { av[0] = mma(wv[gq * 4 + k], bb[gq & 1][k][0], av[0]); av[1] = mma(wv[gq * 4 + k], bb[gq & 1][k][1], av[1]); }
      __builtin_amdgcn_sched_barrier(0);
    }
#pragma unroll
    for (int tk = 0; tk < 2; ++tk)
      *(u32x2*)(VL + (tk * 4 + (w >> 1)) * 1024 + l * 16 + (w & 1) * 8) = u32x2{pk2(av[tk][0], av[tk][1]), pk2(av[tk][2], av[tk][3])};
    bar();
    u32x4 vf[2][4];
#pragma unroll
    for (int tk = 0; tk < 2; ++tk)
#pragma unroll
      for (int ks = 0; ks < 4; ++ks) vf[tk][ks] = *(const u32x4*)(VL + (tk * 4 + ks) * 1024 + l * 16);
    drain();
#pragma unroll
    for (int i = 0; i < 4; ++i) *(u32x4*)(smem + (i * 512 + t) * 16) = pf[i];
#pragma unroll
    for (int ct = 0; ct < 4; ++ct) {
      f32x4 a0 = {0, 0, 0, 0}, a1 = {0, 0, 0, 0};
#pragma unroll
      for (int ks = 0; ks < 4; ++ks) { a0 = mma(vf[0][ks], wo[ct][ks], a0); a1 = mma(vf[1][ks], wo[ct][ks], a1); }
      st16(rs, uOff + (u32)s * (64 * 16384) + ct * 2 * 16384, u32x4{pk2(a0[0], a0[1]), pk2(a0[2], a0[3]), pk2(a1[0], a1[1]), pk2(a1[2], a1[3])});
    }
    if (s <= 2 || s == 63) drain();
    bar();
    if (t == 0) st4(rs, FL_VU + h * FS, (u32)((s <= 2 || s == 63) ? s : s - 1));
  }
}

DI f32x4 reduce8(char* smem, const f32x4 (&acc)[4][2], int w, int l) {
#pragma unroll
  for (int ct = 0; ct < 4; ++ct)
#pragma unroll
    for (int tk = 0; tk < 2; ++tk) *(f32x4*)(smem + ((w * 8 + ct * 2 + tk) * 64 + l) * 16) = acc[ct][tk];
  bar();
  f32x4 p[8];
#pragma unroll
  for (int i = 0; i < 8; ++i) p[i] = *(const f32x4*)(smem + ((i * 8 + w) * 64 + l) * 16);
  return ((p[0] + p[1]) + (p[2] + p[3])) + ((p[4] + p[5]) + (p[6] + p[7]));
}

DI void role_T2(const Args& A, char* smem, rsrc_t rs, int gi, int j) {
  const int t = threadIdx.x, l = t & 63, w = __builtin_amdgcn_readfirstlane(t >> 6);
  const int g = l >> 4, r = l & 15;
  int lim = SPIN_MAX, vudone = -1;
  const u32 uOff = OFF_U + (u32)(8 * j + (r >> 2)) * 16384 + (u32)(4 * w) * 512 + g * 128 + (r & 3) * 16;
  const u32 yOff = OFF_YP + (u32)((4 * w + g) * 512 + 64 * j + 8 * (r >> 2) + (r & 3)) * 16;
  const int ct = w >> 1, tk = w & 1;
  const int ecol = 64 * j + 32 * (ct >> 1) + 8 * g + 4 * (ct & 1);
#pragma unroll
  for (int u = TL0; u < 64; u += 4)
    st8(rs, OFF_H + (u32)(ecol >> 5) * KBS + (u32)(32 * (u + gi) + 16 * tk + r) * 64 + (ecol & 31) * 2, u32x2{SENT, SENT});
  for (int s = gi; s < 64; s += 4) {
    if (vudone < s) { if (w == 0) wait_prog(rs, FL_VU, 32, s, l, vudone, lim); bar(); vudone = s; if (w == 0) vudone = s; }
    u32x4 uf[4][4];
#pragma unroll
    for (int hh = 0; hh < 4; ++hh)
#pragma unroll
      for (int c = 0; c < 4; ++c) uf[hh][c] = ld16(rs, uOff + (u32)s * (64 * 16384) + (c >> 1) * 4 * 16384 + (c & 1) * 64 + hh * 512);
    const int tok = 32 * s + 16 * tk + r;
    u32x2 xv = *(const u32x2*)(A.x + (size_t)tok * 512 + ecol);
    if (w == 0) wait_flags_bk(rs, FL_XP, 32, s >= TL0 ? s - 1 : s, l, lim);
    bar();
    u32x4 pf[4][2], pm[2], yf[4];
    for (int it = 0;; ++it) {
#pragma unroll
      for (int hh = 0; hh < 4; ++hh)
#pragma unroll
        for (int k = 0; k < 2; ++k) pf[hh][k] = ld16(rs, OFF_PT + (u32)((s * 32 + 4 * w + hh) * 2 + k) * 1024 + l * 16);
#pragma unroll
      for (int k = 0; k < 2; ++k) pm[k] = ld16(rs, OFF_PM + (u32)((s * 8 + w) * 2 + k) * 1024 + l * 16);
#pragma unroll
      for (int c = 0; c < 4; ++c) yf[c] = ld16(rs, yOff + (u32)s * (32 * 512 * 16) + ((c >> 1) * 32 + (c & 1) * 4) * 16);
      SB();
      if (s < TL0) break;
      u32 mx = max(mx4(pm[0]), mx4(pm[1]));
#pragma unroll
      for (int c = 0; c < 4; ++c) mx = max(max(mx, mx4(yf[c])), max(mx4(pf[c][0]), mx4(pf[c][1])));
      if (__all(mx != SENT) || it >= lim) break;
      MEMF(); __builtin_amdgcn_s_sleep(4);
    }
    f32x4 acc[4][2] = {};
#pragma unroll
    for (int hh = 0; hh < 4; ++hh)
#pragma unroll
      for (int c = 0; c < 4; ++c) { acc[c][0] = mma(uf[hh][c], pf[hh][0], acc[c][0]); acc[c][1] = mma(uf[hh][c], pf[hh][1], acc[c][1]); }
#pragma unroll
    for (int c = 0; c < 4; ++c) { acc[c][0] = mma(yf[c], pm[0], acc[c][0]); acc[c][1] = mma(yf[c], pm[1], acc[c][1]); }
    f32x4 o = reduce8(smem, acc, w, l);
    u32 a0 = pk2(o[0], o[1]), a1 = pk2(o[2], o[3]);
    u32x2 hv = {nz(pk2(lo16(xv.x) + lo16(a0), hi16(xv.x) + hi16(a0))), nz(pk2(lo16(xv.y) + lo16(a1), hi16(xv.y) + hi16(a1)))};
    st8(rs, OFF_H + (u32)(ecol >> 5) * KBS + (u32)tok * 64 + (ecol & 31) * 2, hv);
    drain();
    bar();
    if (t == 0) st4(rs, FL_T2 + (gi * 8 + j) * FS, (u32)s);
  }
}

template <int MODE>
DI void role_B128(const Args& A, char* smem, rsrc_t rs, int gi, int j, u32 t0) {
  const int t = threadIdx.x, l = t & 63, w = __builtin_amdgcn_readfirstlane(t >> 6);
  const int g = l >> 4, r = l & 15, cg = w & 3, tk = w >> 2;
  int lim = SPIN_MAX;
  u32x4 wf[2][16];
#pragma unroll
  for (int u = 0; u < (64 - TL0) / 2; ++u) {
    const u32 sl = TL0 + 2 * u + gi;
    st16(rs, (MODE == 0 ? OFF_H1 : MODE == 1 ? OFF_X1 : OFF_F) + (u32)(4 * j + cg) * KBS + (u32)(32 * sl + 16 * tk + r) * 64 + g * 16, u32x4{SENT, SENT, SENT, SENT});
    if (MODE == 0) st16(rs, OFF_YB + (u32)(4 * j + (l >> 4)) * KBS + (u32)(32 * sl + 4 * w + ((l >> 2) & 3)) * 64 + (l & 3) * 16, u32x4{SENT, SENT, SENT, SENT});
  }
  hold(t0, MODE == 0 ? BD0 : BD1);
  {
    u32x4 tmp[16];
    const u16* W = (MODE == 0 ? A.w1 : MODE == 1 ? A.w2 : A.w3) + 128 * j;
    slab_load<16, 16>(W, MODE == 2 ? 1792 : 512, t, tmp);
    slab_store<16, 16>(smem, t, tmp);
    __syncthreads();
#pragma unroll
    for (int ks = 0; ks < 16; ++ks)
#pragma unroll
      for (int te = 0; te < 2; ++te) wf[te][ks] = frag_tr<false>(smem, 272, ks * 32, (32 * cg + 8 * (l & 3) + 4 * te) * 2, l);
    __syncthreads();
  }
  const int lg = l & 3, ltk = (l >> 2) & 3, lkq = l >> 4, ltok = 4 * w + ltk;
  const int rslot = (MODE == 1) ? l * 16 : (g * 16 + (r ^ (4 * g))) * 16;
  for (int s = gi; s < 64; s += 2) {
    u32x4 yv;
    if (MODE == 1) {
      if (w == 0) {
        if (s < TL0) {
          wait_flags(rs, FL_B1 + (s & 1) * 4 * FS, 4, s, l, lim, SLPV);
          wait_flags(rs, FL_B1 + ((s + 1) & 1) * 4 * FS, 4, s + 1, l, lim, SLPV);
        } else {
          const int sn = min(s + 1, 63);
          wait_flags(rs, FL_T2 + (sn & 3) * 8 * FS, 8, sn, l, lim, SLPV);
        }
      }
      bar();
      u32x4 v[4];
      for (int it = 0;; ++it) {
#pragma unroll
        for (int i = 0; i < 4; ++i) {
          const int f = 4 * w + i, ks = f & 15, grp = ks >> 2;
          const int sh = (grp == 0) ? -1 : (grp == 1) ? 1 : (grp == 2) ? -2 : 2;
          const int row = 32 * s + 16 * (f >> 4) + r + sh;
          const bool okr = row >= 0 && row < 2048;
          u32x4 z = ld16(rs, OFF_H1 + (u32)ks * KBS + (u32)(okr ? row : 0) * 64 + g * 16);
          v[i] = okr ? z : u32x4{0, 0, 0, 0};
        }
        yv = ld16(rs, OFF_YB + (u32)(4 * j + cg) * KBS + (u32)(32 * s + 16 * tk + r) * 64 + g * 16);
        SB();
        if (s < TL0) break;
        const u32 mx = max(max(max(mx4(v[0]), mx4(v[1])), max(mx4(v[2]), mx4(v[3]))), mx4(yv));
        if (__all(mx != SENT) || it >= lim) break;
        MEMF(); __builtin_amdgcn_s_sleep(4);
      }
#pragma unroll
      for (int i = 0; i < 4; ++i) *(u32x4*)(smem + (4 * w + i) * 1024 + l * 16) = v[i];
    } else {
      const bool last = s >= TL0;
      const int sn = min(s + 1, 63);
      if (w == 0) {
        if (MODE == 0) { if (last) wait_flags(rs, FL_XP, 32, s - 1, l, lim, SLPV); else wait_flags(rs, FL_T2 + (s & 3) * 8 * FS, 8, s, l, lim, SLPV); }
        else { if (last) wait_flags(rs, FL_B1 + (sn & 1) * 4 * FS, 4, sn, l, lim, SLPV); else wait_flags(rs, FL_B2A + gi * 4 * FS, 4, s, l, lim, SLPV); }
      }
      bar();
      u32x4 v[4];
      for (int it = 0;; ++it) {
#pragma unroll
        for (int i = 0; i < 4; ++i) v[i] = ld16(rs, (MODE == 0 ? OFF_H : OFF_X1) + (u32)(4 * i + lkq) * KBS + (u32)(32 * s + ltok) * 64 + lg * 16);
        SB();
        if (!last) break;
        const u32 mx = max(max(mx4(v[0]), mx4(v[1])), max(mx4(v[2]), mx4(v[3])));
        if (__all(mx != SENT) || it >= lim) break;
        MEMF(); __builtin_amdgcn_s_sleep(4);
      }
      f32x2 c[16]; float var;
      ln_center(v, c, var);
      float r1 = __builtin_amdgcn_rsqf(var + 1e-5f), rz = r1;
      if (MODE == 0) {
        rz = r1 * __builtin_amdgcn_rsqf(var * r1 * r1 + 1e-5f);
#pragma unroll
        for (int i = 0; i < 4; ++i)
          if (i == j) st16(rs, OFF_YB + (u32)(4 * i + lkq) * KBS + (u32)(32 * s + ltok) * 64 + lg * 16, nz4(ln_pack(c, i, r1)));
      }
#pragma unroll
      for (int i = 0; i < 4; ++i)
        *(u32x4*)(smem + (((ltok >> 4) * 16 + 4 * i + lkq) * 64 + lg * 16 + ((ltok & 15) ^ (4 * lg))) * 16) = ln_pack(c, i, rz);
    }
    bar();
    f32x4 a0 = {0, 0, 0, 0}, a1 = {0, 0, 0, 0};
    const char* xb = smem + tk * 16384 + rslot;
#pragma unroll
    for (int ks = 0; ks < 16; ++ks) {
      u32x4 b = *(const u32x4*)(xb + ks * 1024);
      a0 = mma(wf[0][ks], b, a0); a1 = mma(wf[1][ks], b, a1);
    }
    u32x4 o;
    if (MODE == 1) {
      u32 p0 = pk2(a0[0], a0[1]), p1 = pk2(a0[2], a0[3]), p2 = pk2(a1[0], a1[1]), p3 = pk2(a1[2], a1[3]);
      o = u32x4{pk2(lo16(yv.x) + lo16(p0), hi16(yv.x) + hi16(p0)), pk2(lo16(yv.y) + lo16(p1), hi16(yv.y) + hi16(p1)),
                pk2(lo16(yv.z) + lo16(p2), hi16(yv.z) + hi16(p2)), pk2(lo16(yv.w) + lo16(p3), hi16(yv.w) + hi16(p3))};
    } else {
      o = u32x4{pk2(gelu_t(a0[0]), gelu_t(a0[1])), pk2(gelu_t(a0[2]), gelu_t(a0[3])), pk2(gelu_t(a1[0]), gelu_t(a1[1])), pk2(gelu_t(a1[2]), gelu_t(a1[3]))};
    }
    st16(rs, (MODE == 0 ? OFF_H1 : MODE == 1 ? OFF_X1 : OFF_F) + (u32)(4 * j + cg) * KBS + (u32)(32 * s + 16 * tk + r) * 64 + g * 16, nz4(o));
    drain();
    bar();
    if (t == 0) st4(rs, (MODE == 0 ? FL_B1 + gi * 4 * FS : MODE == 1 ? FL_B2A + gi * 4 * FS : FL_B2B + gi * 16 * FS) + j * FS, (u32)s);
  }
}

DI void role_B2c(const Args& A, char* smem, rsrc_t rs, int gi, int j, u32 t0) {
  const int t = threadIdx.x, l = t & 63, w = __builtin_amdgcn_readfirstlane(t >> 6);
  const int g = l >> 4, r = l & 15;
  int lim = SPIN_MAX;
  u32x4 wf[4][7];
  hold(t0, BD1);
#pragma unroll
  for (int rnd = 0; rnd < 2; ++rnd) {
    u32x4 tmp[14];
    slab_load<8, 14>(A.w4 + (size_t)rnd * 896 * 512 + 64 * j, 512, t, tmp);
    slab_store<8, 14>(smem, t, tmp);
    __syncthreads();
    if ((w >> 2) == rnd) {
#pragma unroll
      for (int ks = 0; ks < 7; ++ks)
#pragma unroll
        for (int c = 0; c < 4; ++c) wf[c][ks] = frag_tr<false>(smem, 144, (w & 3) * 224 + ks * 32, (32 * (c >> 1) + 8 * (l & 3) + 4 * (c & 1)) * 2, l);
    }
    __syncthreads();
  }
  const int ct = w >> 1, tk = w & 1;
  const int ecol = 64 * j + 32 * (ct >> 1) + 8 * g + 4 * (ct & 1);
  for (int s = gi; s < 64; s += 2) {
    if (w == 0) {
      if (s >= TL0) wait_flags(rs, FL_B2A + gi * 4 * FS, 4, s, l, lim, SLPV);
      else wait_flags(rs, FL_B2B + gi * 16 * FS, 14, s, l, lim, SLPV);
    }
    bar();
    u32x4 ff[2][7];
    const int tok = 32 * s + 16 * tk + r;
    const u32 eo = (u32)(ecol >> 5) * KBS + (u32)tok * 64 + (ecol & 31) * 2;
    u32x2 hv, xv;
    for (int it = 0;; ++it) {
#pragma unroll
      for (int k = 0; k < 2; ++k)
#pragma unroll
        for (int ks = 0; ks < 7; ++ks) ff[k][ks] = ld16(rs, OFF_F + (u32)(7 * w + ks) * KBS + (u32)(32 * s + 16 * k + r) * 64 + g * 16);
      hv = ld8(rs, OFF_H + eo); xv = ld8(rs, OFF_X1 + eo);
      SB();
      if (s < TL0) break;
      u32 mx = 0;
#pragma unroll
      for (int ks = 0; ks < 7; ++ks) mx = max(mx, max(mx4(ff[0][ks]), mx4(ff[1][ks])));
      if (__all(mx != SENT) || it >= lim) break;
      MEMF(); __builtin_amdgcn_s_sleep(4);
    }
    f32x4 acc[4][2] = {};
#pragma unroll
    for (int ks = 0; ks < 7; ++ks)
#pragma unroll
      for (int c = 0; c < 4; ++c) { acc[c][0] = mma(wf[c][ks], ff[0][ks], acc[c][0]); acc[c][1] = mma(wf[c][ks], ff[1][ks], acc[c][1]); }
    f32x4 o = reduce8(smem, acc, w, l);
    u32 a0 = pk2(o[0], o[1]), a1 = pk2(o[2], o[3]);
    u32 b0 = pk2(lo16(xv.x) + lo16(a0), hi16(xv.x) + hi16(a0)), b1 = pk2(lo16(xv.y) + lo16(a1), hi16(xv.y) + hi16(a1));
    u32x2 ov = {pk2(lo16(hv.x) + lo16(b0), hi16(hv.x) + hi16(b0)), pk2(lo16(hv.y) + lo16(b1), hi16(hv.y) + hi16(b1))};
    *(u32x2*)(A.out + (size_t)tok * 512 + ecol) = ov;
    bar();
  }
}

DI void calib(rsrc_t rs, char* ws) {
  if (threadIdx.x == 0) {
    u32 res0 = 0xFFFFFFFFu, res1 = 0xFFFFFFFFu;
    for (int rep = 0; rep < 10; ++rep) {
      u32* q = (u32*)(ws + OFF_COMM + (rep & 1) * 8192); u32 v = 0xFFFFFFFFu;
      asm volatile("global_store_dword %0, %1, off sc1\n\ts_waitcnt vmcnt(0)" :: "v"(q), "v"(v) : "memory");
      unsigned long long c1 = __builtin_amdgcn_s_memtime(); u32 r;
      asm volatile("global_load_dword %0, %1, off sc1\n\ts_waitcnt vmcnt(0)" : "=v"(r) : "v"(q) : "memory");
      unsigned long long c2 = __builtin_amdgcn_s_memtime();
      u32 d = (u32)(c2 - c1) + (r & 0);
      if (rep & 1) res1 = min(res1, d); else res0 = min(res0, d);
    }
    st4(rs, FL_SEL, res0 < res1 ? 0u : 1u);
  }
}

__global__ __launch_bounds__(512, 2) void k_mega(Args A) {
  extern __shared__ __attribute__((aligned(16))) char smem[];
  const int b = blockIdx.x, xcd = b & 7, id = (b >> 3) * 4 + (xcd & 3);
  rsrc_t rs = __builtin_amdgcn_make_buffer_rsrc((void*)A.ws, 0, WS_USED, 0x00020000);
  const u32 t0 = (u32)__builtin_amdgcn_s_memrealtime();
  if (xcd < 4) {
    if (id < 32) role_X(A, smem, rs, id, xcd, t0);
    else if (id < 96) role_Y(A, smem, rs, id - 32, xcd);
    else {
      const int a = id - 96;
      if (a == 8) calib(rs, A.ws);
      ln_task(A, rs, a);
      if (MODEV & 64) return;
      if (a < 8) role_B128<0>(A, smem, rs, a >> 2, a & 3, t0);
      else if (a < 16) role_B128<1>(A, smem, rs, (a - 8) >> 2, a & 3, t0);
      else role_B2c(A, smem, rs, (a - 16) >> 3, a & 7, t0);
    }
  } else {
    if (id < 32) role_QK(A, smem, rs, id);
    else if (id < 64) role_VU(A, smem, rs, id - 32);
    else {
      fold_task(A, smem, rs, ((16 + (id & 3) + 4 * ((id - 64) >> 4)) << 2) | ((id >> 2) & 3));
      if (MODEV & 64) return;
      if (id < 96) role_T2(A, smem, rs, (id - 64) >> 3, id & 7);
      else if (id < 124) role_B128<2>(A, smem, rs, (id - 96) / 14, (id - 96) % 14, t0);
    }
  }
}

extern "C" void kernel_launch(void* const* d_in, const int* in_sizes, int n_in, void* d_out, int out_size, void* d_ws, size_t ws_size, hipStream_t stream) {
  Args A;
  A.x = (const u16*)d_in[0]; A.mem0 = (const u16*)d_in[1];
  A.Wq = (const u16*)d_in[2]; A.Wk = (const u16*)d_in[3]; A.Wv = (const u16*)d_in[4]; A.Wo = (const u16*)d_in[5];
  A.w1 = (const u16*)d_in[8]; A.w2 = (const u16*)d_in[9]; A.w3 = (const u16*)d_in[10]; A.w4 = (const u16*)d_in[11];
  A.out = (u16*)d_out;
  char* ws = (char*)(((uintptr_t)d_ws + 2 * MiB - 1) & ~(uintptr_t)(2 * MiB - 1));
  A.ws = ws;
  hipMemsetAsync(ws, 0xFF, CTRL_BYTES, stream);
  hipFuncSetAttribute((const void*)k_mega, hipFuncAttributeMaxDynamicSharedMemorySize, LDS_BYTES);
  k_mega<<<256, 512, LDS_BYTES, stream>>>(A);
}
```
